```python
import torch

def _softmax(x, axis=-1):
    return torch.softmax(x, dim=axis)

def _gelu(x):
    return torch.nn.functional.gelu(x, approximate='tanh')

def _silu(x):
    return torch.nn.functional.silu(x)

def _sigmoid(x):
    return torch.sigmoid(x)

def _layernorm(x, axis=-1, eps=1e-05):
    m = x.mean(dim=axis, keepdim=True)
    v = x.var(dim=axis, keepdim=True, unbiased=False)
    return (x - m) / torch.sqrt(v + eps)

def _scatter_add(tgt, idx, src, axis=0):
    return tgt.clone().index_add_(axis, idx, src)

def _segment_sum(src, seg, n):
    out = torch.zeros((n,) + src.shape[1:], dtype=src.dtype)
    return out.index_add_(0, seg, src)

def _pad(x, pw):
    flat = tuple((v for pair in reversed(pw) for v in pair))
    return torch.nn.functional.pad(x, flat)
kernel_name = 'standalone__dilated_atrous__vision'

def setup_inputs():

    _w = lambda a: (lambda t: t.to(torch.bfloat16) if t.is_floating_point() else t)(torch.as_tensor(a))
    H = W = 384
    C = 96
    k = 3
    return {'x': _w((lambda *s: torch.randn(*s, dtype=torch.bfloat16) * 0.05)(1, H, W, C)), 'w': _w((lambda *s: torch.randn(*s, dtype=torch.bfloat16) * 0.05)(k, k, C, C) * (1.0 / (k * C ** 0.5)))}

def reference(x, w):
    B, H, W, C = x.shape
    k = 3
    rate = 2
    pad = (k - 1) * rate // 2
    xp = _pad(x, ((0, 0), (pad, pad), (pad, pad), (0, 0)))
    out = torch.zeros_like(x)
    for i in range(k):
        for j in range(k):
            tap = xp[:, i * rate:i * rate + H, j * rate:j * rate + W, :].clone()
            out = out + torch.einsum('bhwc,co->bhwo', tap, w[i, j])
    return out
```

```cpp
#include <hip/hip_runtime.h>
#include <hip/hip_bf16.h>
#include <stdint.h>

#define TOTAL_BYTES 28311552u
#define RB 73728u
#define X_SHIFT (2u * RB + 384u + 64u)

__global__ __launch_bounds__(256, 1)
void dilated_conv_kernel(const unsigned short* __restrict__ x, const unsigned short* __restrict__ w,
                         unsigned short* __restrict__ out) {
  __shared__ __attribute__((aligned(16))) char lds[163840];
  const unsigned tid = threadIdx.x;
  const unsigned ldsb = (unsigned)(size_t)lds;
  unsigned vwp, vw0, vw1, vxp, vx0, vx1, vx2, vx3, vx4, vl16;
  unsigned mh, nh, m0w, m0x, xso, xsof, xsol, oso, sbase;
  asm volatile(
    "s_getpc_b64 s[40:41]\n\t"
    ".Lconv_code_pc:\n\t"
    "v_lshrrev_b32 v254, 6, %[tid]\n\t"
    "v_and_b32 v255, 7, %[tid]\n\t"
    "s_lshr_b32 s42, %[bid], 3\n\t"
    "v_and_b32 v253, 63, %[tid]\n\t"
    "v_readfirstlane_b32 s43, v254\n\t"
    "s_lshl_b32 s44, s42, 10\n\t"
    "v_lshl_add_u32 v255, v255, 7, s44\n\t"
    "v_lshlrev_b32 %[vl16], 4, v253\n\t"
    "s_and_b32 %[mh], s43, 1\n\t"
    "s_lshr_b32 %[nh], s43, 1\n\t"
    "v_min_u32 v255, (.Lconv_code_end-.Lconv_code_pc-4)&0xfffffffc, v255\n\t"
    "s_mul_i32 s44, %[mh], 320\n\t"
    "s_mul_i32 s45, %[mh], 9216\n\t"
    "global_load_dword v255, v255, s[40:41]\n\t"
    "v_add_u32 v252, s44, v253\n\t"
    "s_mul_i32 s46, %[nh], 1536\n\t"
    "s_mul_i32 s47, %[mh], 5120\n\t"
    "v_mul_u32_u24 v251, 0x556, v252\n\t"
    "s_add_u32 s45, s45, s46\n\t"
    "s_add_u32 s44, s47, 64\n\t"
    "v_and_b32 v254, 32, v253\n\t"
    "v_add_u32 %[vw0], s45, %[vl16]\n\t"
    "v_add_u32 v250, s44, %[vl16]\n\t"
    "v_mul_u32_u24 v254, 48, v254\n\t"
    "v_lshrrev_b32 %[vx0], 11, v251\n\t"
    "v_add_u32 %[vx1], 87424, v251\n\t"
    "v_add_u32 %[vx2], 174848, v251\n\t"
    "v_add_u32 %[vx3], 262272, v251\n\t"
    "v_add_u32 %[vx4], 349696, v251\n\t"
    "v_add_u32 %[vw1], %[vw0], v254\n\t"
    "s_mul_i32 %[m0x], %[nh], 10240\n\t"
    "v_bitop3_b32 %[vx0], v250, %[vx0], 32 bitop3:0x78\n\t"
    "v_lshrrev_b32 %[vx1], 11, %[vx1]\n\t"
    "v_lshrrev_b32 %[vx2], 11, %[vx2]\n\t"
    "v_lshrrev_b32 %[vx3], 11, %[vx3]\n\t"
    "v_lshrrev_b32 %[vx4], 11, %[vx4]\n\t"
    "s_add_u32 %[m0x], %[m0x], s47\n\t"
    "s_mul_i32 %[m0w], %[nh], 10248\n\t"
    "s_mul_i32 s46, %[mh], 4608\n\t"
    "v_bitop3_b32 %[vx1], v250, %[vx1], 32 bitop3:0x78\n\t"
    "v_bitop3_b32 %[vx2], v250, %[vx2], 32 bitop3:0x78\n\t"
    "v_bitop3_b32 %[vx3], v250, %[vx3], 32 bitop3:0x78\n\t"
    "v_bitop3_b32 %[vx4], v250, %[vx4], 32 bitop3:0x78\n\t"
    "s_add_u32 %[m0w], %[m0w], s46\n\t"
    "s_add_u32 %[m0x], %[m0x], %[ldsb]\n\t"
    "s_add_u32 %[m0w], %[m0w], %[ldsb]\n\t"
    "v_bfrev_b32 v254, 1\n\t"
    "s_and_b32 s44, s42, 7\n\t"
    "s_and_b32 s45, %[bid], 3\n\t"
    "s_lshr_b32 s46, %[bid], 6\n\t"
    "s_lshl2_add_u32 s45, s45, s46\n\t"
    "s_bfe_u32 s46, %[bid], 0x10002\n\t"
    "s_cmp_eq_u32 s44, 0\n\t"
    "s_cselect_b32 s47, 24, 0\n\t"
    "v_add_u32 %[vx4], 0x1000, %[vx4]\n\t"
    "v_cmp_le_u32 vcc, s47, v252\n\t"
    "s_mul_i32 s42, s45, 24\n\t"
    "s_add_u32 s42, s42, s46\n\t"
    "s_mul_i32 s42, s42, 73728\n\t"
    "v_cndmask_b32 %[vx0], v254, %[vx0], vcc\n\t"
    "s_cmp_eq_u32 s44, 7\n\t"
    "s_movk_i32 s47, 368\n\t"
    "s_cselect_b32 s47, 344, s47\n\t"
    "s_mul_i32 s46, s44, 9216\n\t"
    "v_cmp_gt_u32 vcc, s47, v252\n\t"
    "s_add_u32 s42, s42, s46\n\t"
    "s_mul_i32 s46, %[nh], 884736\n\t"
    "s_mul_i32 s47, %[mh], 884736\n\t"
    "v_cndmask_b32 %[vx4], v254, %[vx4], vcc\n\t"
    "s_add_u32 %[xso], s42, s46\n\t"
    "s_add_u32 %[oso], s42, s47\n\t"
    "s_add_u32 s46, s45, %[nh]\n\t"
    "s_cmp_eq_u32 s46, 0\n\t"
    "s_cselect_b32 %[xsof], 0x40000000, %[xso]\n\t"
    "s_mul_i32 s47, %[nh], 147456\n\t"
    "s_add_u32 s47, s47, 1769472\n\t"
    "s_add_u32 s47, s47, s42\n\t"
    "s_cmp_eq_u32 s46, 16\n\t"
    "s_cselect_b32 %[xsol], 0x40000000, s47\n\t"
    "s_lshr_b32 s46, %[bid], 3\n\t"
    "s_lshl2_add_u32 s46, s46, s43\n\t"
    "v_min_u32 v254, 10, v253\n\t"
    "s_mul_i32 s46, s46, 1408\n\t"
    "v_lshl_add_u32 %[vwp], v254, 7, s46\n\t"
    "s_mul_i32 s46, %[mh], 4608\n\t"
    "v_min_u32 v254, 35, v253\n\t"
    "s_add_u32 s46, s46, 448\n\t"
    "v_lshl_add_u32 %[vxp], v254, 7, s46\n\t"
    "s_mul_i32 %[sbase], %[mh], 10240\n\t"
    "s_add_u32 %[sbase], %[sbase], %[ldsb]\n\t"
    : [vwp] "=&v"(vwp), [vw0] "=&v"(vw0), [vw1] "=&v"(vw1), [vxp] "=&v"(vxp), [vx0] "=&v"(vx0), [vx1] "=&v"(vx1), [vx2] "=&v"(vx2), [vx3] "=&v"(vx3), [vx4] "=&v"(vx4),
      [vl16] "=&v"(vl16), [mh] "=&s"(mh), [nh] "=&s"(nh), [m0w] "=&s"(m0w), [m0x] "=&s"(m0x), [xso] "=&s"(xso), [xsof] "=&s"(xsof),
      [xsol] "=&s"(xsol), [oso] "=&s"(oso), [sbase] "=&s"(sbase)
    : [tid] "v"(tid), [bid] "s"(blockIdx.x), [ldsb] "s"(ldsb)
    : "v250", "v251", "v252", "v253", "v254", "v255", "s40", "s41", "s42", "s43", "s44", "s45", "s46", "s47", "vcc", "scc", "memory");
  __builtin_amdgcn_sched_barrier(0);
  __amdgpu_buffer_rsrc_t srdw = __builtin_amdgcn_make_buffer_rsrc((void*)w, (short)0, 165888, 0x00020000);
  __amdgpu_buffer_rsrc_t srdx = __builtin_amdgcn_make_buffer_rsrc((void*)((const char*)x - X_SHIFT), (short)0, (int)(TOTAL_BYTES + X_SHIFT), 0x00020000);
  __amdgpu_buffer_rsrc_t srdo = __builtin_amdgcn_make_buffer_rsrc((void*)out, (short)0, (int)TOTAL_BYTES, 0x00020000);

  asm volatile(
    "s_nop 4\n\t"
    "s_mov_b32 s40, 0\n\t"
    "s_add_u32 m0, %[m0w], 61440\n\t"
    "s_nop 0\n\t"
    "buffer_load_dwordx4 %[vw0], %[srdw], s40 offen lds\n\t"
    "buffer_load_dwordx4 %[vw1], %[srdw], s40 offen offset:1024 lds\n\t"
    "s_mov_b32 s40, 1536\n\t"
    "buffer_load_dwordx4 %[vw0], %[srdw], s40 offen offset:2048 lds\n\t"
    "s_mov_b32 s40, 3072\n\t"
    "buffer_load_dwordx4 %[vw0], %[srdw], s40 offen offset:3072 lds\n\t"
    "s_mov_b32 s40, 7168\n\t"
    "s_add_u32 m0, %[m0w], 65536\n\t"
    "s_mov_b32 exec_hi, 0\n\t"
    "buffer_load_dwordx4 %[vw0], %[srdw], s40 offen lds\n\t"
    "s_mov_b32 exec_hi, -1\n\t"
    "s_add_u32 s44, %[xsof], 0\n\t"
    "s_add_u32 m0, %[m0x], 0\n\t"
    "s_nop 0\n\t"
    "buffer_load_dwordx4 %[vx0], %[srdx], s44 offen offset:0 lds\n\t"
    "v_and_b32 v22, 0xf0, %[vl16]\n\t"
    "v_lshrrev_b32 v21, 4, %[vl16]\n\t"
    "v_mul_u32_u24 v10, 12, v22\n\t"
    "v_and_b32 v20, 48, v21\n\t"
    "v_lshrrev_b32 v22, 1, v22\n\t"
    "buffer_load_dwordx4 %[vx1], %[srdx], s44 offen offset:1024 lds\n\t"
    "v_add_u32 v10, v10, v20\n\t"
    "v_bfe_u32 v19, v21, 4, 1\n\t"
    "v_and_b32 v20, 44, v21\n\t"
    "v_bitop3_b32 v11, v10, v22, 32 bitop3:0x78\n\t"
    "v_mul_u32_u24 v19, 10248, v19\n\t"
    "buffer_load_dwordx4 %[vx2], %[srdx], s44 offen offset:2048 lds\n\t"
    "v_mul_u32_u24 v20, 48, v20\n\t"
    "v_add_u32 v12, 384, v10\n\t"
    "v_add_u32 v18, 16, v22\n\t"
    "v_and_b32 v21, 3, v21\n\t"
    "v_add_u32 v13, 768, v10\n\t"
    "buffer_load_dwordx4 %[vx3], %[srdx], s44 offen offset:3072 lds\n\t"
    "v_add_u32 v22, 32, v22\n\t"
    "v_add_u32 v19, v19, v20\n\t"
    "v_bitop3_b32 v12, v12, v18, 32 bitop3:0x78\n\t"
    "v_bitop3_b32 v13, v13, v22, 32 bitop3:0x78\n\t"
    "v_lshl_add_u32 v19, v21, 4, v19\n\t"
    "s_add_u32 m0, %[m0x], 4096\n\t"
    "s_nop 0\n\t"
    "buffer_load_dwordx4 %[vx4], %[srdx], s44 offen lds\n\t"
    "s_lshl_b32 s41, %[nh], 7\n\t"
    "v_add_u32 v15, s41, v19\n\t"
    "s_lshl_b32 s41, %[nh], 3\n\t"
    "s_add_u32 s41, s41, 64\n\t"
    "v_add_u32 v14, s41, v19\n\t"
    "s_mov_b32 s40, 18432\n\t"
    "s_add_u32 m0, %[m0w], 81920\n\t"
    "s_nop 0\n\t"
    "buffer_load_dwordx4 %[vw0], %[srdw], s40 offen lds\n\t"
    "s_mul_i32 s45, %[mh], 10240\n\t"
    "s_add_u32 s45, s45, 0\n\t"
    "s_mul_i32 s46, %[mh], 10240\n\t"
    "s_add_u32 s46, s46, 20480\n\t"
    "s_mul_i32 s47, %[mh], 10240\n\t"
    "buffer_load_dwordx4 %[vw1], %[srdw], s40 offen offset:1024 lds\n\t"
    "s_add_u32 s47, s47, 40960\n\t"
    "s_mul_i32 s48, %[mh], 10240\n\t"
    "s_add_u32 s48, s48, 143360\n\t"
    "s_mul_i32 s49, %[mh], 10240\n\t"
    "s_add_u32 s49, s49, 61440\n\t"
    "s_mov_b32 s40, 19968\n\t"
    "buffer_load_dwordx4 %[vw0], %[srdw], s40 offen offset:2048 lds\n\t"
    "s_mul_i32 s50, %[mh], 10240\n\t"
    "s_add_u32 s50, s50, 81920\n\t"
    "s_mul_i32 s51, %[mh], 92160\n\t"
    "s_add_u32 s51, s51, 10240\n\t"
    "s_mul_i32 s52, %[mh], 81920\n\t"
    "s_mov_b32 s40, 21504\n\t"
    "buffer_load_dwordx4 %[vw0], %[srdw], s40 offen offset:3072 lds\n\t"
    "s_add_u32 s52, s52, 30720\n\t"
    "s_mov_b32 s40, 25600\n\t"
    "s_add_u32 m0, %[m0w], 86016\n\t"
    "s_mov_b32 exec_hi, 0\n\t"
    "buffer_load_dwordx4 %[vw0], %[srdw], s40 offen lds\n\t"
    "s_mov_b32 exec_hi, -1\n\t"
    "buffer_load_dword v24, %[vwp], %[srdw], 0 offen\n\t"
    "s_waitcnt vmcnt(11)\n\t"
    "s_barrier\n\t"
    "v_add_u32 v32, 61440, v15\n\t"
    "v_add_u32 v33, 61440, v14\n\t"
    "ds_read_b64_tr_b16 a[0:1], v32 offset:0\n\t"
    "ds_read_b64_tr_b16 a[2:3], v32 offset:768\n\t"
    "ds_read_b64_tr_b16 a[12:13], v32 offset:3072\n\t"
    "ds_read_b64_tr_b16 a[14:15], v32 offset:3840\n\t"
    "ds_read_b64_tr_b16 a[24:25], v32 offset:6144\n\t"
    "ds_read_b64_tr_b16 a[26:27], v32 offset:6912\n\t"
    "ds_read_b64_tr_b16 a[4:5], v32 offset:8\n\t"
    "ds_read_b64_tr_b16 a[6:7], v32 offset:776\n\t"
    "ds_read_b64_tr_b16 a[16:17], v32 offset:3080\n\t"
    "ds_read_b64_tr_b16 a[18:19], v32 offset:3848\n\t"
    "ds_read_b64_tr_b16 a[28:29], v32 offset:6152\n\t"
    "ds_read_b64_tr_b16 a[30:31], v32 offset:6920\n\t"
    "ds_read_b64_tr_b16 a[8:9], v33 offset:0\n\t"
    "ds_read_b64_tr_b16 a[10:11], v33 offset:768\n\t"
    "ds_read_b64_tr_b16 a[20:21], v33 offset:3072\n\t"
    "ds_read_b64_tr_b16 a[22:23], v33 offset:3840\n\t"
    "ds_read_b64_tr_b16 a[32:33], v33 offset:6144\n\t"
    "ds_read_b64_tr_b16 a[34:35], v33 offset:6912\n\t"
    "s_mov_b32 s40, 36864\n\t"
    "s_add_u32 m0, %[m0w], 102400\n\t"
    "s_nop 0\n\t"
    "buffer_load_dwordx4 %[vw0], %[srdw], s40 offen lds\n\t"
    "buffer_load_dwordx4 %[vw1], %[srdw], s40 offen offset:1024 lds\n\t"
    "s_mov_b32 s40, 38400\n\t"
    "buffer_load_dwordx4 %[vw0], %[srdw], s40 offen offset:2048 lds\n\t"
    "s_mov_b32 s40, 39936\n\t"
    "buffer_load_dwordx4 %[vw0], %[srdw], s40 offen offset:3072 lds\n\t"
    "s_mov_b32 s40, 44032\n\t"
    "s_add_u32 m0, %[m0w], 106496\n\t"
    "s_mov_b32 exec_hi, 0\n\t"
    "buffer_load_dwordx4 %[vw0], %[srdw], s40 offen lds\n\t"
    "s_mov_b32 exec_hi, -1\n\t"
    "s_add_u32 s41, %[xso], 147456\n\t"
    "buffer_load_dword v25, %[vxp], %[srdx], s41 offen\n\t"
    "s_waitcnt vmcnt(7) lgkmcnt(0)\n\t"
    "s_barrier\n\t"
    "v_add_u32 v34, s45, v11\n\t"
    "ds_read_b128 v[44:47], v34 offset:0\n\t"
    "ds_read_b128 v[48:51], v34 offset:64\n\t"
    "ds_read_b128 v[52:55], v34 offset:128\n\t"
    "ds_read_b128 v[56:59], v34 offset:3072\n\t"
    "ds_read_b128 v[60:63], v34 offset:3136\n\t"
    "ds_read_b128 v[64:67], v34 offset:3200\n\t"
    "; ---- unit 0: row 0 j 0 b 0 ds [0]\n\t"
    "s_waitcnt lgkmcnt(3)\n\t"
    "v_mfma_f32_16x16x32_bf16 v[80:83], a[0:3], v[44:47], 0\n\t"
    "ds_read_b128 v[68:71], v34 offset:6144\n\t"
    "ds_read_b128 v[72:75], v34 offset:6208\n\t"
    "s_add_u32 s44, %[xso], 147456\n\t"
    "v_mfma_f32_16x16x32_bf16 v[80:83], a[12:15], v[48:51], v[80:83]\n\t"
    "ds_read_b128 v[76:79], v34 offset:6272\n\t"
    "s_add_u32 m0, %[m0x], 20480\n\t"
    "v_mfma_f32_16x16x32_bf16 v[80:83], a[24:27], v[52:55], v[80:83]\n\t"
    "v_add_u32 v32, 81920, v15\n\t"
    "buffer_load_dwordx4 %[vx0], %[srdx], s44 offen offset:0 lds\n\t"
    "v_mfma_f32_16x16x32_bf16 v[84:87], a[4:7], v[44:47], 0\n\t"
    "v_add_u32 v33, 81920, v14\n\t"
    "buffer_load_dwordx4 %[vx1], %[srdx], s44 offen offset:1024 lds\n\t"
    "v_mfma_f32_16x16x32_bf16 v[84:87], a[16:19], v[48:51], v[84:87]\n\t"
    "ds_read_b64_tr_b16 a[36:37], v32 offset:0\n\t"
    "buffer_load_dwordx4 %[vx2], %[srdx], s44 offen offset:2048 lds\n\t"
    "v_mfma_f32_16x16x32_bf16 v[84:87], a[28:31], v[52:55], v[84:87]\n\t"
    "ds_read_b64_tr_b16 a[38:39], v32 offset:768\n\t"
    "buffer_load_dwordx4 %[vx3], %[srdx], s44 offen offset:3072 lds\n\t"
    "v_mfma_f32_16x16x32_bf16 v[88:91], a[8:11], v[44:47], 0\n\t"
    "ds_read_b64_tr_b16 a[48:49], v32 offset:3072\n\t"
    "s_add_u32 m0, %[m0x], 24576\n\t"
    "v_mfma_f32_16x16x32_bf16 v[88:91], a[20:23], v[48:51], v[88:91]\n\t"
    "ds_read_b64_tr_b16 a[50:51], v32 offset:3840\n\t"
    "buffer_load_dwordx4 %[vx4], %[srdx], s44 offen lds\n\t"
    "v_mfma_f32_16x16x32_bf16 v[88:91], a[32:35], v[52:55], v[88:91]\n\t"
    "ds_read_b64_tr_b16 a[60:61], v32 offset:6144\n\t"
    "; ---- unit 1: row 0 j 0 b 1 ds [0]\n\t"
    "s_waitcnt lgkmcnt(8)\n\t"
    "v_mfma_f32_16x16x32_bf16 v[92:95], a[0:3], v[56:59], 0\n\t"
    "v_add_u32 v35, s45, v12\n\t"
    "ds_read_b128 v[44:47], v35 offset:0\n\t"
    "v_mfma_f32_16x16x32_bf16 v[92:95], a[12:15], v[60:63], v[92:95]\n\t"
    "ds_read_b128 v[48:51], v35 offset:64\n\t"
    "v_mfma_f32_16x16x32_bf16 v[92:95], a[24:27], v[64:67], v[92:95]\n\t"
    "ds_read_b128 v[52:55], v35 offset:128\n\t"
    "v_mfma_f32_16x16x32_bf16 v[96:99], a[4:7], v[56:59], 0\n\t"
    "ds_read_b64_tr_b16 a[62:63], v32 offset:6912\n\t"
    "v_mfma_f32_16x16x32_bf16 v[96:99], a[16:19], v[60:63], v[96:99]\n\t"
    "ds_read_b64_tr_b16 a[40:41], v32 offset:8\n\t"
    "ds_read_b64_tr_b16 a[42:43], v32 offset:776\n\t"
    "v_mfma_f32_16x16x32_bf16 v[96:99], a[28:31], v[64:67], v[96:99]\n\t"
    "ds_read_b64_tr_b16 a[52:53], v32 offset:3080\n\t"
    "v_mfma_f32_16x16x32_bf16 v[100:103], a[8:11], v[56:59], 0\n\t"
    "ds_read_b64_tr_b16 a[54:55], v32 offset:3848\n\t"
    "v_mfma_f32_16x16x32_bf16 v[100:103], a[20:23], v[60:63], v[100:103]\n\t"
    "ds_read_b64_tr_b16 a[64:65], v32 offset:6152\n\t"
    "v_mfma_f32_16x16x32_bf16 v[100:103], a[32:35], v[64:67], v[100:103]\n\t"
    "ds_read_b64_tr_b16 a[66:67], v32 offset:6920\n\t"
    "; ---- unit 2: row 0 j 0 b 2 ds [0]\n\t"
    "s_waitcnt lgkmcnt(15)\n\t"
    "v_mfma_f32_16x16x32_bf16 v[104:107], a[0:3], v[68:71], 0\n\t"
    "ds_read_b128 v[56:59], v35 offset:3072\n\t"
    "v_mfma_f32_16x16x32_bf16 v[104:107], a[12:15], v[72:75], v[104:107]\n\t"
    "ds_read_b128 v[60:63], v35 offset:3136\n\t"
    "v_mfma_f32_16x16x32_bf16 v[104:107], a[24:27], v[76:79], v[104:107]\n\t"
    "ds_read_b128 v[64:67], v35 offset:3200\n\t"
    "v_mfma_f32_16x16x32_bf16 v[108:111], a[4:7], v[68:71], 0\n\t"
    "ds_read_b64_tr_b16 a[44:45], v33 offset:0\n\t"
    "v_mfma_f32_16x16x32_bf16 v[108:111], a[16:19], v[72:75], v[108:111]\n\t"
    "ds_read_b64_tr_b16 a[46:47], v33 offset:768\n\t"
    "v_mfma_f32_16x16x32_bf16 v[108:111], a[28:31], v[76:79], v[108:111]\n\t"
    "ds_read_b64_tr_b16 a[56:57], v33 offset:3072\n\t"
    "v_mfma_f32_16x16x32_bf16 v[112:115], a[8:11], v[68:71], 0\n\t"
    "ds_read_b64_tr_b16 a[58:59], v33 offset:3840\n\t"
    "v_mfma_f32_16x16x32_bf16 v[112:115], a[20:23], v[72:75], v[112:115]\n\t"
    "ds_read_b64_tr_b16 a[68:69], v33 offset:6144\n\t"
    "v_mfma_f32_16x16x32_bf16 v[112:115], a[32:35], v[76:79], v[112:115]\n\t"
    "ds_read_b64_tr_b16 a[70:71], v33 offset:6912\n\t"
    "; ---- unit 3: row 0 j 1 b 0 ds [0]\n\t"
    "s_waitcnt vmcnt(6) lgkmcnt(0)\n\t"
    "s_barrier\n\t"
    "v_mfma_f32_16x16x32_bf16 v[80:83], a[36:39], v[44:47], v[80:83]\n\t"
    "ds_read_b128 v[68:71], v35 offset:6144\n\t"
    "ds_read_b128 v[72:75], v35 offset:6208\n\t"
    "s_mov_b32 s40, 55296\n\t"
    "v_mfma_f32_16x16x32_bf16 v[80:83], a[48:51], v[48:51], v[80:83]\n\t"
    "ds_read_b128 v[76:79], v35 offset:6272\n\t"
    "s_add_u32 m0, %[m0w], 122880\n\t"
    "v_mfma_f32_16x16x32_bf16 v[80:83], a[60:63], v[52:55], v[80:83]\n\t"
    "v_add_u32 v32, 102400, v15\n\t"
    "buffer_load_dwordx4 %[vw0], %[srdw], s40 offen lds\n\t"
    "v_mfma_f32_16x16x32_bf16 v[84:87], a[40:43], v[44:47], v[84:87]\n\t"
    "v_add_u32 v33, 102400, v14\n\t"
    "buffer_load_dwordx4 %[vw1], %[srdw], s40 offen offset:1024 lds\n\t"
    "v_mfma_f32_16x16x32_bf16 v[84:87], a[52:55], v[48:51], v[84:87]\n\t"
    "ds_read_b64_tr_b16 a[72:73], v32 offset:0\n\t"
    "s_mov_b32 s40, 56832\n\t"
    "v_mfma_f32_16x16x32_bf16 v[84:87], a[64:67], v[52:55], v[84:87]\n\t"
    "ds_read_b64_tr_b16 a[74:75], v32 offset:768\n\t"
    "buffer_load_dwordx4 %[vw0], %[srdw], s40 offen offset:2048 lds\n\t"
    "v_mfma_f32_16x16x32_bf16 v[88:91], a[44:47], v[44:47], v[88:91]\n\t"
    "ds_read_b64_tr_b16 a[84:85], v32 offset:3072\n\t"
    "s_mov_b32 s40, 58368\n\t"
    "v_mfma_f32_16x16x32_bf16 v[88:91], a[56:59], v[48:51], v[88:91]\n\t"
    "ds_read_b64_tr_b16 a[86:87], v32 offset:3840\n\t"
    "buffer_load_dwordx4 %[vw0], %[srdw], s40 offen offset:3072 lds\n\t"
    "v_mfma_f32_16x16x32_bf16 v[88:91], a[68:71], v[52:55], v[88:91]\n\t"
    "ds_read_b64_tr_b16 a[96:97], v32 offset:6144\n\t"
    "s_mov_b32 s40, 62464\n\t"
    "; ---- unit 4: row 0 j 1 b 1 ds [0]\n\t"
    "v_mfma_f32_16x16x32_bf16 v[92:95], a[36:39], v[56:59], v[92:95]\n\t"
    "v_add_u32 v36, s45, v13\n\t"
    "ds_read_b128 v[44:47], v36 offset:0\n\t"
    "s_add_u32 m0, %[m0w], 126976\n\t"
    "v_mfma_f32_16x16x32_bf16 v[92:95], a[48:51], v[60:63], v[92:95]\n\t"
    "ds_read_b128 v[48:51], v36 offset:64\n\t"
    "s_mov_b32 exec_hi, 0\n\t"
    "buffer_load_dwordx4 %[vw0], %[srdw], s40 offen lds\n\t"
    "s_mov_b32 exec_hi, -1\n\t"
    "v_mfma_f32_16x16x32_bf16 v[92:95], a[60:63], v[64:67], v[92:95]\n\t"
    "ds_read_b128 v[52:55], v36 offset:128\n\t"
    "v_mfma_f32_16x16x32_bf16 v[96:99], a[40:43], v[56:59], v[96:99]\n\t"
    "ds_read_b64_tr_b16 a[98:99], v32 offset:6912\n\t"
    "v_mfma_f32_16x16x32_bf16 v[96:99], a[52:55], v[60:63], v[96:99]\n\t"
    "ds_read_b64_tr_b16 a[76:77], v32 offset:8\n\t"
    "ds_read_b64_tr_b16 a[78:79], v32 offset:776\n\t"
    "v_mfma_f32_16x16x32_bf16 v[96:99], a[64:67], v[64:67], v[96:99]\n\t"
    "ds_read_b64_tr_b16 a[88:89], v32 offset:3080\n\t"
    "v_mfma_f32_16x16x32_bf16 v[100:103], a[44:47], v[56:59], v[100:103]\n\t"
    "ds_read_b64_tr_b16 a[90:91], v32 offset:3848\n\t"
    "v_mfma_f32_16x16x32_bf16 v[100:103], a[56:59], v[60:63], v[100:103]\n\t"
    "ds_read_b64_tr_b16 a[100:101], v32 offset:6152\n\t"
    "v_mfma_f32_16x16x32_bf16 v[100:103], a[68:71], v[64:67], v[100:103]\n\t"
    "ds_read_b64_tr_b16 a[102:103], v32 offset:6920\n\t"
    "; ---- unit 5: row 0 j 1 b 2 ds [0]\n\t"
    "s_waitcnt lgkmcnt(15)\n\t"
    "v_mfma_f32_16x16x32_bf16 v[104:107], a[36:39], v[68:71], v[104:107]\n\t"
    "ds_read_b128 v[56:59], v36 offset:3072\n\t"
    "v_mfma_f32_16x16x32_bf16 v[104:107], a[48:51], v[72:75], v[104:107]\n\t"
    "ds_read_b128 v[60:63], v36 offset:3136\n\t"
    "v_mfma_f32_16x16x32_bf16 v[104:107], a[60:63], v[76:79], v[104:107]\n\t"
    "ds_read_b128 v[64:67], v36 offset:3200\n\t"
    "v_mfma_f32_16x16x32_bf16 v[108:111], a[40:43], v[68:71], v[108:111]\n\t"
    "ds_read_b64_tr_b16 a[80:81], v33 offset:0\n\t"
    "v_mfma_f32_16x16x32_bf16 v[108:111], a[52:55], v[72:75], v[108:111]\n\t"
    "ds_read_b64_tr_b16 a[82:83], v33 offset:768\n\t"
    "v_mfma_f32_16x16x32_bf16 v[108:111], a[64:67], v[76:79], v[108:111]\n\t"
    "ds_read_b64_tr_b16 a[92:93], v33 offset:3072\n\t"
    "v_mfma_f32_16x16x32_bf16 v[112:115], a[44:47], v[68:71], v[112:115]\n\t"
    "ds_read_b64_tr_b16 a[94:95], v33 offset:3840\n\t"
    "v_mfma_f32_16x16x32_bf16 v[112:115], a[56:59], v[72:75], v[112:115]\n\t"
    "ds_read_b64_tr_b16 a[104:105], v33 offset:6144\n\t"
    "v_mfma_f32_16x16x32_bf16 v[112:115], a[68:71], v[76:79], v[112:115]\n\t"
    "ds_read_b64_tr_b16 a[106:107], v33 offset:6912\n\t"
    "; ---- unit 6: row 0 j 2 b 0 ds [0]\n\t"
    "s_waitcnt lgkmcnt(15)\n\t"
    "v_mfma_f32_16x16x32_bf16 v[80:83], a[72:75], v[44:47], v[80:83]\n\t"
    "ds_read_b128 v[68:71], v36 offset:6144\n\t"
    "v_mfma_f32_16x16x32_bf16 v[80:83], a[84:87], v[48:51], v[80:83]\n\t"
    "v_mfma_f32_16x16x32_bf16 v[80:83], a[96:99], v[52:55], v[80:83]\n\t"
    "s_waitcnt lgkmcnt(10)\n\t"
    "v_mfma_f32_16x16x32_bf16 v[84:87], a[76:79], v[44:47], v[84:87]\n\t"
    "ds_read_b128 v[72:75], v36 offset:6208\n\t"
    "v_mfma_f32_16x16x32_bf16 v[84:87], a[88:91], v[48:51], v[84:87]\n\t"
    "v_mfma_f32_16x16x32_bf16 v[84:87], a[100:103], v[52:55], v[84:87]\n\t"
    "s_waitcnt lgkmcnt(2)\n\t"
    "v_mfma_f32_16x16x32_bf16 v[88:91], a[80:83], v[44:47], v[88:91]\n\t"
    "ds_read_b128 v[76:79], v36 offset:6272\n\t"
    "v_mfma_f32_16x16x32_bf16 v[88:91], a[92:95], v[48:51], v[88:91]\n\t"
    "v_mfma_f32_16x16x32_bf16 v[88:91], a[104:107], v[52:55], v[88:91]\n\t"
    "; ---- unit 7: row 0 j 2 b 1 ds [0]\n\t"
    "s_waitcnt vmcnt(5)\n\t"
    "s_barrier\n\t"
    "v_mfma_f32_16x16x32_bf16 v[92:95], a[72:75], v[56:59], v[92:95]\n\t"
    "v_add_u32 v37, s46, v11\n\t"
    "v_mfma_f32_16x16x32_bf16 v[92:95], a[84:87], v[60:63], v[92:95]\n\t"
    "s_mov_b32 s40, 73728\n\t"
    "v_mfma_f32_16x16x32_bf16 v[92:95], a[96:99], v[64:67], v[92:95]\n\t"
    "ds_read_b128 v[44:47], v37 offset:0\n\t"
    "v_mfma_f32_16x16x32_bf16 v[96:99], a[76:79], v[56:59], v[96:99]\n\t"
    "s_add_u32 m0, %[m0w], 143360\n\t"
    "v_mfma_f32_16x16x32_bf16 v[96:99], a[88:91], v[60:63], v[96:99]\n\t"
    "ds_read_b128 v[48:51], v37 offset:64\n\t"
    "v_mfma_f32_16x16x32_bf16 v[96:99], a[100:103], v[64:67], v[96:99]\n\t"
    "buffer_load_dwordx4 %[vw0], %[srdw], s40 offen lds\n\t"
    "v_mfma_f32_16x16x32_bf16 v[100:103], a[80:83], v[56:59], v[100:103]\n\t"
    "ds_read_b128 v[52:55], v37 offset:128\n\t"
    "v_mfma_f32_16x16x32_bf16 v[100:103], a[92:95], v[60:63], v[100:103]\n\t"
    "buffer_load_dwordx4 %[vw1], %[srdw], s40 offen offset:1024 lds\n\t"
    "v_mfma_f32_16x16x32_bf16 v[100:103], a[104:107], v[64:67], v[100:103]\n\t"
    "s_mov_b32 s40, 75264\n\t"
    "; ---- unit 8: row 0 j 2 b 2 ds [0]\n\t"
    "s_waitcnt lgkmcnt(3)\n\t"
    "v_mfma_f32_16x16x32_bf16 v[104:107], a[72:75], v[68:71], v[104:107]\n\t"
    "ds_read_b128 v[56:59], v37 offset:3072\n\t"
    "v_mfma_f32_16x16x32_bf16 v[104:107], a[84:87], v[72:75], v[104:107]\n\t"
    "buffer_load_dwordx4 %[vw0], %[srdw], s40 offen offset:2048 lds\n\t"
    "v_mfma_f32_16x16x32_bf16 v[104:107], a[96:99], v[76:79], v[104:107]\n\t"
    "s_mov_b32 s40, 76800\n\t"
    "v_mfma_f32_16x16x32_bf16 v[108:111], a[76:79], v[68:71], v[108:111]\n\t"
    "ds_read_b128 v[60:63], v37 offset:3136\n\t"
    "v_mfma_f32_16x16x32_bf16 v[108:111], a[88:91], v[72:75], v[108:111]\n\t"
    "buffer_load_dwordx4 %[vw0], %[srdw], s40 offen offset:3072 lds\n\t"
    "v_mfma_f32_16x16x32_bf16 v[108:111], a[100:103], v[76:79], v[108:111]\n\t"
    "s_mov_b32 s40, 80896\n\t"
    "v_mfma_f32_16x16x32_bf16 v[112:115], a[80:83], v[68:71], v[112:115]\n\t"
    "ds_read_b128 v[64:67], v37 offset:3200\n\t"
    "v_mfma_f32_16x16x32_bf16 v[112:115], a[92:95], v[72:75], v[112:115]\n\t"
    "s_add_u32 m0, %[m0w], 147456\n\t"
    "v_mfma_f32_16x16x32_bf16 v[112:115], a[104:107], v[76:79], v[112:115]\n\t"
    "s_mov_b32 exec_hi, 0\n\t"
    "buffer_load_dwordx4 %[vw0], %[srdw], s40 offen lds\n\t"
    "s_mov_b32 exec_hi, -1\n\t"
    "; ---- unit 9: row 1 j 0 b 0 ds [0]\n\t"
    "s_waitcnt lgkmcnt(3)\n\t"
    "v_mfma_f32_16x16x32_bf16 v[116:119], a[0:3], v[44:47], 0\n\t"
    "ds_read_b128 v[68:71], v37 offset:6144\n\t"
    "v_mfma_f32_16x16x32_bf16 v[116:119], a[12:15], v[48:51], v[116:119]\n\t"
    "v_mfma_f32_16x16x32_bf16 v[116:119], a[24:27], v[52:55], v[116:119]\n\t"
    "v_mfma_f32_16x16x32_bf16 v[120:123], a[4:7], v[44:47], 0\n\t"
    "ds_read_b128 v[72:75], v37 offset:6208\n\t"
    "v_mfma_f32_16x16x32_bf16 v[120:123], a[16:19], v[48:51], v[120:123]\n\t"
    "v_mfma_f32_16x16x32_bf16 v[120:123], a[28:31], v[52:55], v[120:123]\n\t"
    "v_mfma_f32_16x16x32_bf16 v[124:127], a[8:11], v[44:47], 0\n\t"
    "ds_read_b128 v[76:79], v37 offset:6272\n\t"
    "v_mfma_f32_16x16x32_bf16 v[124:127], a[20:23], v[48:51], v[124:127]\n\t"
    "v_mfma_f32_16x16x32_bf16 v[124:127], a[32:35], v[52:55], v[124:127]\n\t"
    "; ---- unit 10: row 1 j 0 b 1 ds [0]\n\t"
    "s_waitcnt lgkmcnt(3)\n\t"
    "v_mfma_f32_16x16x32_bf16 v[128:131], a[0:3], v[56:59], 0\n\t"
    "v_add_u32 v34, s46, v12\n\t"
    "v_mfma_f32_16x16x32_bf16 v[128:131], a[12:15], v[60:63], v[128:131]\n\t"
    "s_mov_b32 s40, 92160\n\t"
    "v_mfma_f32_16x16x32_bf16 v[128:131], a[24:27], v[64:67], v[128:131]\n\t"
    "ds_read_b128 v[44:47], v34 offset:0\n\t"
    "v_mfma_f32_16x16x32_bf16 v[132:135], a[4:7], v[56:59], 0\n\t"
    "s_add_u32 m0, %[m0w], 61440\n\t"
    "v_mfma_f32_16x16x32_bf16 v[132:135], a[16:19], v[60:63], v[132:135]\n\t"
    "ds_read_b128 v[48:51], v34 offset:64\n\t"
    "v_mfma_f32_16x16x32_bf16 v[132:135], a[28:31], v[64:67], v[132:135]\n\t"
    "buffer_load_dwordx4 %[vw0], %[srdw], s40 offen lds\n\t"
    "v_mfma_f32_16x16x32_bf16 v[136:139], a[8:11], v[56:59], 0\n\t"
    "ds_read_b128 v[52:55], v34 offset:128\n\t"
    "v_mfma_f32_16x16x32_bf16 v[136:139], a[20:23], v[60:63], v[136:139]\n\t"
    "buffer_load_dwordx4 %[vw1], %[srdw], s40 offen offset:1024 lds\n\t"
    "v_mfma_f32_16x16x32_bf16 v[136:139], a[32:35], v[64:67], v[136:139]\n\t"
    "s_mov_b32 s40, 93696\n\t"
    "; ---- unit 11: row 1 j 0 b 2 ds [0]\n\t"
    "s_waitcnt lgkmcnt(3)\n\t"
    "v_mfma_f32_16x16x32_bf16 v[140:143], a[0:3], v[68:71], 0\n\t"
    "ds_read_b128 v[56:59], v34 offset:3072\n\t"
    "v_mfma_f32_16x16x32_bf16 v[140:143], a[12:15], v[72:75], v[140:143]\n\t"
    "buffer_load_dwordx4 %[vw0], %[srdw], s40 offen offset:2048 lds\n\t"
    "v_mfma_f32_16x16x32_bf16 v[140:143], a[24:27], v[76:79], v[140:143]\n\t"
    "s_mov_b32 s40, 95232\n\t"
    "v_mfma_f32_16x16x32_bf16 v[144:147], a[4:7], v[68:71], 0\n\t"
    "ds_read_b128 v[60:63], v34 offset:3136\n\t"
    "v_mfma_f32_16x16x32_bf16 v[144:147], a[16:19], v[72:75], v[144:147]\n\t"
    "buffer_load_dwordx4 %[vw0], %[srdw], s40 offen offset:3072 lds\n\t"
    "v_mfma_f32_16x16x32_bf16 v[144:147], a[28:31], v[76:79], v[144:147]\n\t"
    "s_mov_b32 s40, 99328\n\t"
    "v_mfma_f32_16x16x32_bf16 v[148:151], a[8:11], v[68:71], 0\n\t"
    "ds_read_b128 v[64:67], v34 offset:3200\n\t"
    "v_mfma_f32_16x16x32_bf16 v[148:151], a[20:23], v[72:75], v[148:151]\n\t"
    "s_add_u32 m0, %[m0w], 65536\n\t"
    "v_mfma_f32_16x16x32_bf16 v[148:151], a[32:35], v[76:79], v[148:151]\n\t"
    "s_mov_b32 exec_hi, 0\n\t"
    "buffer_load_dwordx4 %[vw0], %[srdw], s40 offen lds\n\t"
    "s_mov_b32 exec_hi, -1\n\t"
    "; ---- unit 12: row 1 j 1 b 0 ds [0]\n\t"
    "s_waitcnt vmcnt(10)\n\t"
    "s_barrier\n\t"
    "s_waitcnt lgkmcnt(3)\n\t"
    "v_mfma_f32_16x16x32_bf16 v[116:119], a[36:39], v[44:47], v[116:119]\n\t"
    "ds_read_b128 v[68:71], v34 offset:6144\n\t"
    "ds_read_b128 v[72:75], v34 offset:6208\n\t"
    "v_mfma_f32_16x16x32_bf16 v[116:119], a[48:51], v[48:51], v[116:119]\n\t"
    "ds_read_b128 v[76:79], v34 offset:6272\n\t"
    "v_mfma_f32_16x16x32_bf16 v[116:119], a[60:63], v[52:55], v[116:119]\n\t"
    "v_add_u32 v32, 122880, v15\n\t"
    "v_mfma_f32_16x16x32_bf16 v[120:123], a[40:43], v[44:47], v[120:123]\n\t"
    "v_add_u32 v33, 122880, v14\n\t"
    "v_mfma_f32_16x16x32_bf16 v[120:123], a[52:55], v[48:51], v[120:123]\n\t"
    "ds_read_b64_tr_b16 a[108:109], v32 offset:0\n\t"
    "v_mfma_f32_16x16x32_bf16 v[120:123], a[64:67], v[52:55], v[120:123]\n\t"
    "ds_read_b64_tr_b16 a[110:111], v32 offset:768\n\t"
    "v_mfma_f32_16x16x32_bf16 v[124:127], a[44:47], v[44:47], v[124:127]\n\t"
    "ds_read_b64_tr_b16 a[120:121], v32 offset:3072\n\t"
    "v_mfma_f32_16x16x32_bf16 v[124:127], a[56:59], v[48:51], v[124:127]\n\t"
    "ds_read_b64_tr_b16 a[122:123], v32 offset:3840\n\t"
    "v_mfma_f32_16x16x32_bf16 v[124:127], a[68:71], v[52:55], v[124:127]\n\t"
    "ds_read_b64_tr_b16 a[132:133], v32 offset:6144\n\t"
    "; ---- unit 13: row 1 j 1 b 1 ds [0]\n\t"
    "s_waitcnt lgkmcnt(8)\n\t"
    "v_mfma_f32_16x16x32_bf16 v[128:131], a[36:39], v[56:59], v[128:131]\n\t"
    "v_add_u32 v35, s46, v13\n\t"
    "ds_read_b128 v[44:47], v35 offset:0\n\t"
    "v_mfma_f32_16x16x32_bf16 v[128:131], a[48:51], v[60:63], v[128:131]\n\t"
    "ds_read_b128 v[48:51], v35 offset:64\n\t"
    "v_mfma_f32_16x16x32_bf16 v[128:131], a[60:63], v[64:67], v[128:131]\n\t"
    "ds_read_b128 v[52:55], v35 offset:128\n\t"
    "v_mfma_f32_16x16x32_bf16 v[132:135], a[40:43], v[56:59], v[132:135]\n\t"
    "ds_read_b64_tr_b16 a[134:135], v32 offset:6912\n\t"
    "v_mfma_f32_16x16x32_bf16 v[132:135], a[52:55], v[60:63], v[132:135]\n\t"
    "ds_read_b64_tr_b16 a[112:113], v32 offset:8\n\t"
    "ds_read_b64_tr_b16 a[114:115], v32 offset:776\n\t"
    "v_mfma_f32_16x16x32_bf16 v[132:135], a[64:67], v[64:67], v[132:135]\n\t"
    "ds_read_b64_tr_b16 a[124:125], v32 offset:3080\n\t"
    "v_mfma_f32_16x16x32_bf16 v[136:139], a[44:47], v[56:59], v[136:139]\n\t"
    "ds_read_b64_tr_b16 a[126:127], v32 offset:3848\n\t"
    "v_mfma_f32_16x16x32_bf16 v[136:139], a[56:59], v[60:63], v[136:139]\n\t"
    "ds_read_b64_tr_b16 a[136:137], v32 offset:6152\n\t"
    "v_mfma_f32_16x16x32_bf16 v[136:139], a[68:71], v[64:67], v[136:139]\n\t"
    "ds_read_b64_tr_b16 a[138:139], v32 offset:6920\n\t"
    "; ---- unit 14: row 1 j 1 b 2 ds [0]\n\t"
    "s_waitcnt lgkmcnt(15)\n\t"
    "v_mfma_f32_16x16x32_bf16 v[140:143], a[36:39], v[68:71], v[140:143]\n\t"
    "ds_read_b128 v[56:59], v35 offset:3072\n\t"
    "v_mfma_f32_16x16x32_bf16 v[140:143], a[48:51], v[72:75], v[140:143]\n\t"
    "ds_read_b128 v[60:63], v35 offset:3136\n\t"
    "v_mfma_f32_16x16x32_bf16 v[140:143], a[60:63], v[76:79], v[140:143]\n\t"
    "ds_read_b128 v[64:67], v35 offset:3200\n\t"
    "v_mfma_f32_16x16x32_bf16 v[144:147], a[40:43], v[68:71], v[144:147]\n\t"
    "ds_read_b64_tr_b16 a[116:117], v33 offset:0\n\t"
    "v_mfma_f32_16x16x32_bf16 v[144:147], a[52:55], v[72:75], v[144:147]\n\t"
    "ds_read_b64_tr_b16 a[118:119], v33 offset:768\n\t"
    "v_mfma_f32_16x16x32_bf16 v[144:147], a[64:67], v[76:79], v[144:147]\n\t"
    "ds_read_b64_tr_b16 a[128:129], v33 offset:3072\n\t"
    "v_mfma_f32_16x16x32_bf16 v[148:151], a[44:47], v[68:71], v[148:151]\n\t"
    "ds_read_b64_tr_b16 a[130:131], v33 offset:3840\n\t"
    "v_mfma_f32_16x16x32_bf16 v[148:151], a[56:59], v[72:75], v[148:151]\n\t"
    "ds_read_b64_tr_b16 a[140:141], v33 offset:6144\n\t"
    "v_mfma_f32_16x16x32_bf16 v[148:151], a[68:71], v[76:79], v[148:151]\n\t"
    "ds_read_b64_tr_b16 a[142:143], v33 offset:6912\n\t"
    "; ---- unit 15: row 1 j 2 b 0 ds [0]\n\t"
    "s_waitcnt vmcnt(5) lgkmcnt(0)\n\t"
    "s_barrier\n\t"
    "v_mfma_f32_16x16x32_bf16 v[116:119], a[72:75], v[44:47], v[116:119]\n\t"
    "ds_read_b128 v[68:71], v35 offset:6144\n\t"
    "ds_read_b128 v[72:75], v35 offset:6208\n\t"
    "s_add_u32 s44, %[xso], 294912\n\t"
    "v_mfma_f32_16x16x32_bf16 v[116:119], a[84:87], v[48:51], v[116:119]\n\t"
    "ds_read_b128 v[76:79], v35 offset:6272\n\t"
    "s_add_u32 m0, %[m0x], 40960\n\t"
    "v_mfma_f32_16x16x32_bf16 v[116:119], a[96:99], v[52:55], v[116:119]\n\t"
    "v_add_u32 v32, 143360, v15\n\t"
    "buffer_load_dwordx4 %[vx0], %[srdx], s44 offen offset:0 lds\n\t"
    "v_mfma_f32_16x16x32_bf16 v[120:123], a[76:79], v[44:47], v[120:123]\n\t"
    "v_add_u32 v33, 143360, v14\n\t"
    "buffer_load_dwordx4 %[vx1], %[srdx], s44 offen offset:1024 lds\n\t"
    "v_mfma_f32_16x16x32_bf16 v[120:123], a[88:91], v[48:51], v[120:123]\n\t"
    "ds_read_b64_tr_b16 a[144:145], v32 offset:0\n\t"
    "buffer_load_dwordx4 %[vx2], %[srdx], s44 offen offset:2048 lds\n\t"
    "v_mfma_f32_16x16x32_bf16 v[120:123], a[100:103], v[52:55], v[120:123]\n\t"
    "ds_read_b64_tr_b16 a[146:147], v32 offset:768\n\t"
    "buffer_load_dwordx4 %[vx3], %[srdx], s44 offen offset:3072 lds\n\t"
    "v_mfma_f32_16x16x32_bf16 v[124:127], a[80:83], v[44:47], v[124:127]\n\t"
    "ds_read_b64_tr_b16 a[156:157], v32 offset:3072\n\t"
    "s_add_u32 m0, %[m0x], 45056\n\t"
    "v_mfma_f32_16x16x32_bf16 v[124:127], a[92:95], v[48:51], v[124:127]\n\t"
    "ds_read_b64_tr_b16 a[158:159], v32 offset:3840\n\t"
    "buffer_load_dwordx4 %[vx4], %[srdx], s44 offen lds\n\t"
    "v_mfma_f32_16x16x32_bf16 v[124:127], a[104:107], v[52:55], v[124:127]\n\t"
    "ds_read_b64_tr_b16 a[168:169], v32 offset:6144\n\t"
    "; ---- unit 16: row 1 j 2 b 1 ds [0]\n\t"
    "v_mfma_f32_16x16x32_bf16 v[128:131], a[72:75], v[56:59], v[128:131]\n\t"
    "ds_read_b128 v[44:47], v37 offset:0\n\t"
    "ds_read_b128 v[48:51], v37 offset:64\n\t"
    "v_mfma_f32_16x16x32_bf16 v[128:131], a[84:87], v[60:63], v[128:131]\n\t"
    "ds_read_b128 v[52:55], v37 offset:128\n\t"
    "v_mfma_f32_16x16x32_bf16 v[128:131], a[96:99], v[64:67], v[128:131]\n\t"
    "ds_read_b64_tr_b16 a[170:171], v32 offset:6912\n\t"
    "v_mfma_f32_16x16x32_bf16 v[132:135], a[76:79], v[56:59], v[132:135]\n\t"
    "ds_read_b64_tr_b16 a[148:149], v32 offset:8\n\t"
    "v_mfma_f32_16x16x32_bf16 v[132:135], a[88:91], v[60:63], v[132:135]\n\t"
    "ds_read_b64_tr_b16 a[150:151], v32 offset:776\n\t"
    "v_mfma_f32_16x16x32_bf16 v[132:135], a[100:103], v[64:67], v[132:135]\n\t"
    "ds_read_b64_tr_b16 a[160:161], v32 offset:3080\n\t"
    "v_mfma_f32_16x16x32_bf16 v[136:139], a[80:83], v[56:59], v[136:139]\n\t"
    "ds_read_b64_tr_b16 a[162:163], v32 offset:3848\n\t"
    "v_mfma_f32_16x16x32_bf16 v[136:139], a[92:95], v[60:63], v[136:139]\n\t"
    "ds_read_b64_tr_b16 a[172:173], v32 offset:6152\n\t"
    "v_mfma_f32_16x16x32_bf16 v[136:139], a[104:107], v[64:67], v[136:139]\n\t"
    "ds_read_b64_tr_b16 a[174:175], v32 offset:6920\n\t"
    "; ---- unit 17: row 1 j 2 b 2 ds [0]\n\t"
    "s_waitcnt lgkmcnt(15)\n\t"
    "v_mfma_f32_16x16x32_bf16 v[140:143], a[72:75], v[68:71], v[140:143]\n\t"
    "ds_read_b128 v[56:59], v37 offset:3072\n\t"
    "v_mfma_f32_16x16x32_bf16 v[140:143], a[84:87], v[72:75], v[140:143]\n\t"
    "ds_read_b128 v[60:63], v37 offset:3136\n\t"
    "v_mfma_f32_16x16x32_bf16 v[140:143], a[96:99], v[76:79], v[140:143]\n\t"
    "ds_read_b128 v[64:67], v37 offset:3200\n\t"
    "v_mfma_f32_16x16x32_bf16 v[144:147], a[76:79], v[68:71], v[144:147]\n\t"
    "ds_read_b64_tr_b16 a[152:153], v33 offset:0\n\t"
    "v_mfma_f32_16x16x32_bf16 v[144:147], a[88:91], v[72:75], v[144:147]\n\t"
    "ds_read_b64_tr_b16 a[154:155], v33 offset:768\n\t"
    "v_mfma_f32_16x16x32_bf16 v[144:147], a[100:103], v[76:79], v[144:147]\n\t"
    "ds_read_b64_tr_b16 a[164:165], v33 offset:3072\n\t"
    "v_mfma_f32_16x16x32_bf16 v[148:151], a[80:83], v[68:71], v[148:151]\n\t"
    "ds_read_b64_tr_b16 a[166:167], v33 offset:3840\n\t"
    "v_mfma_f32_16x16x32_bf16 v[148:151], a[92:95], v[72:75], v[148:151]\n\t"
    "ds_read_b64_tr_b16 a[176:177], v33 offset:6144\n\t"
    "v_mfma_f32_16x16x32_bf16 v[148:151], a[104:107], v[76:79], v[148:151]\n\t"
    "ds_read_b64_tr_b16 a[178:179], v33 offset:6912\n\t"
    "; ---- unit 18: row 1 j 0 b 0 ds [1]\n\t"
    "s_waitcnt lgkmcnt(15)\n\t"
    "v_mfma_f32_16x16x32_bf16 v[80:83], a[108:111], v[44:47], v[80:83]\n\t"
    "ds_read_b128 v[68:71], v37 offset:6144\n\t"
    "v_mfma_f32_16x16x32_bf16 v[80:83], a[120:123], v[48:51], v[80:83]\n\t"
    "v_mfma_f32_16x16x32_bf16 v[80:83], a[132:135], v[52:55], v[80:83]\n\t"
    "v_mfma_f32_16x16x32_bf16 v[84:87], a[112:115], v[44:47], v[84:87]\n\t"
    "ds_read_b128 v[72:75], v37 offset:6208\n\t"
    "v_mfma_f32_16x16x32_bf16 v[84:87], a[124:127], v[48:51], v[84:87]\n\t"
    "v_mfma_f32_16x16x32_bf16 v[84:87], a[136:139], v[52:55], v[84:87]\n\t"
    "v_mfma_f32_16x16x32_bf16 v[88:91], a[116:119], v[44:47], v[88:91]\n\t"
    "ds_read_b128 v[76:79], v37 offset:6272\n\t"
    "v_mfma_f32_16x16x32_bf16 v[88:91], a[128:131], v[48:51], v[88:91]\n\t"
    "v_mfma_f32_16x16x32_bf16 v[88:91], a[140:143], v[52:55], v[88:91]\n\t"
    "; ---- unit 19: row 1 j 0 b 1 ds [1]\n\t"
    "s_waitcnt lgkmcnt(9)\n\t"
    "v_mfma_f32_16x16x32_bf16 v[92:95], a[108:111], v[56:59], v[92:95]\n\t"
    "ds_read_b128 v[44:47], v34 offset:0\n\t"
    "v_mfma_f32_16x16x32_bf16 v[92:95], a[120:123], v[60:63], v[92:95]\n\t"
    "v_mfma_f32_16x16x32_bf16 v[92:95], a[132:135], v[64:67], v[92:95]\n\t"
    "v_mfma_f32_16x16x32_bf16 v[96:99], a[112:115], v[56:59], v[96:99]\n\t"
    "ds_read_b128 v[48:51], v34 offset:64\n\t"
    "v_mfma_f32_16x16x32_bf16 v[96:99], a[124:127], v[60:63], v[96:99]\n\t"
    "v_mfma_f32_16x16x32_bf16 v[96:99], a[136:139], v[64:67], v[96:99]\n\t"
    "v_mfma_f32_16x16x32_bf16 v[100:103], a[116:119], v[56:59], v[100:103]\n\t"
    "ds_read_b128 v[52:55], v34 offset:128\n\t"
    "v_mfma_f32_16x16x32_bf16 v[100:103], a[128:131], v[60:63], v[100:103]\n\t"
    "v_mfma_f32_16x16x32_bf16 v[100:103], a[140:143], v[64:67], v[100:103]\n\t"
    "; ---- unit 20: row 1 j 0 b 2 ds [1]\n\t"
    "s_waitcnt lgkmcnt(3)\n\t"
    "v_mfma_f32_16x16x32_bf16 v[104:107], a[108:111], v[68:71], v[104:107]\n\t"
    "ds_read_b128 v[56:59], v34 offset:3072\n\t"
    "v_mfma_f32_16x16x32_bf16 v[104:107], a[120:123], v[72:75], v[104:107]\n\t"
    "v_mfma_f32_16x16x32_bf16 v[104:107], a[132:135], v[76:79], v[104:107]\n\t"
    "v_mfma_f32_16x16x32_bf16 v[108:111], a[112:115], v[68:71], v[108:111]\n\t"
    "ds_read_b128 v[60:63], v34 offset:3136\n\t"
    "v_mfma_f32_16x16x32_bf16 v[108:111], a[124:127], v[72:75], v[108:111]\n\t"
    "v_mfma_f32_16x16x32_bf16 v[108:111], a[136:139], v[76:79], v[108:111]\n\t"
    "v_mfma_f32_16x16x32_bf16 v[112:115], a[116:119], v[68:71], v[112:115]\n\t"
    "ds_read_b128 v[64:67], v34 offset:3200\n\t"
    "v_mfma_f32_16x16x32_bf16 v[112:115], a[128:131], v[72:75], v[112:115]\n\t"
    "v_mfma_f32_16x16x32_bf16 v[112:115], a[140:143], v[76:79], v[112:115]\n\t"
    "; ---- unit 21: row 1 j 1 b 0 ds [1]\n\t"
    "s_waitcnt vmcnt(5)\n\t"
    "s_barrier\n\t"
    "s_waitcnt lgkmcnt(3)\n\t"
    "v_mfma_f32_16x16x32_bf16 v[80:83], a[144:147], v[44:47], v[80:83]\n\t"
    "ds_read_b128 v[68:71], v34 offset:6144\n\t"
    "ds_read_b128 v[72:75], v34 offset:6208\n\t"
    "s_mov_b32 s40, 110592\n\t"
    "v_mfma_f32_16x16x32_bf16 v[80:83], a[156:159], v[48:51], v[80:83]\n\t"
    "ds_read_b128 v[76:79], v34 offset:6272\n\t"
    "s_add_u32 m0, %[m0w], 81920\n\t"
    "v_mfma_f32_16x16x32_bf16 v[80:83], a[168:171], v[52:55], v[80:83]\n\t"
    "v_add_u32 v32, 61440, v15\n\t"
    "buffer_load_dwordx4 %[vw0], %[srdw], s40 offen lds\n\t"
    "v_mfma_f32_16x16x32_bf16 v[84:87], a[148:151], v[44:47], v[84:87]\n\t"
    "v_add_u32 v33, 61440, v14\n\t"
    "buffer_load_dwordx4 %[vw1], %[srdw], s40 offen offset:1024 lds\n\t"
    "v_mfma_f32_16x16x32_bf16 v[84:87], a[160:163], v[48:51], v[84:87]\n\t"
    "ds_read_b64_tr_b16 a[180:181], v32 offset:0\n\t"
    "s_mov_b32 s40, 112128\n\t"
    "v_mfma_f32_16x16x32_bf16 v[84:87], a[172:175], v[52:55], v[84:87]\n\t"
    "ds_read_b64_tr_b16 a[182:183], v32 offset:768\n\t"
    "buffer_load_dwordx4 %[vw0], %[srdw], s40 offen offset:2048 lds\n\t"
    "v_mfma_f32_16x16x32_bf16 v[88:91], a[152:155], v[44:47], v[88:91]\n\t"
    "ds_read_b64_tr_b16 a[192:193], v32 offset:3072\n\t"
    "s_mov_b32 s40, 113664\n\t"
    "v_mfma_f32_16x16x32_bf16 v[88:91], a[164:167], v[48:51], v[88:91]\n\t"
    "ds_read_b64_tr_b16 a[194:195], v32 offset:3840\n\t"
    "buffer_load_dwordx4 %[vw0], %[srdw], s40 offen offset:3072 lds\n\t"
    "v_mfma_f32_16x16x32_bf16 v[88:91], a[176:179], v[52:55], v[88:91]\n\t"
    "ds_read_b64_tr_b16 a[204:205], v32 offset:6144\n\t"
    "s_mov_b32 s40, 117760\n\t"
    "; ---- unit 22: row 1 j 1 b 1 ds [1]\n\t"
    "s_waitcnt lgkmcnt(8)\n\t"
    "v_mfma_f32_16x16x32_bf16 v[92:95], a[144:147], v[56:59], v[92:95]\n\t"
    "ds_read_b128 v[44:47], v35 offset:0\n\t"
    "ds_read_b128 v[48:51], v35 offset:64\n\t"
    "s_add_u32 m0, %[m0w], 86016\n\t"
    "v_mfma_f32_16x16x32_bf16 v[92:95], a[156:159], v[60:63], v[92:95]\n\t"
    "ds_read_b128 v[52:55], v35 offset:128\n\t"
    "s_mov_b32 exec_hi, 0\n\t"
    "buffer_load_dwordx4 %[vw0], %[srdw], s40 offen lds\n\t"
    "s_mov_b32 exec_hi, -1\n\t"
    "v_mfma_f32_16x16x32_bf16 v[92:95], a[168:171], v[64:67], v[92:95]\n\t"
    "ds_read_b64_tr_b16 a[206:207], v32 offset:6912\n\t"
    "v_mfma_f32_16x16x32_bf16 v[96:99], a[148:151], v[56:59], v[96:99]\n\t"
    "ds_read_b64_tr_b16 a[184:185], v32 offset:8\n\t"
    "v_mfma_f32_16x16x32_bf16 v[96:99], a[160:163], v[60:63], v[96:99]\n\t"
    "ds_read_b64_tr_b16 a[186:187], v32 offset:776\n\t"
    "v_mfma_f32_16x16x32_bf16 v[96:99], a[172:175], v[64:67], v[96:99]\n\t"
    "ds_read_b64_tr_b16 a[196:197], v32 offset:3080\n\t"
    "v_mfma_f32_16x16x32_bf16 v[100:103], a[152:155], v[56:59], v[100:103]\n\t"
    "ds_read_b64_tr_b16 a[198:199], v32 offset:3848\n\t"
    "v_mfma_f32_16x16x32_bf16 v[100:103], a[164:167], v[60:63], v[100:103]\n\t"
    "ds_read_b64_tr_b16 a[208:209], v32 offset:6152\n\t"
    "v_mfma_f32_16x16x32_bf16 v[100:103], a[176:179], v[64:67], v[100:103]\n\t"
    "ds_read_b64_tr_b16 a[210:211], v32 offset:6920\n\t"
    "; ---- unit 23: row 1 j 1 b 2 ds [1]\n\t"
    "s_waitcnt lgkmcnt(15)\n\t"
    "v_mfma_f32_16x16x32_bf16 v[104:107], a[144:147], v[68:71], v[104:107]\n\t"
    "ds_read_b128 v[56:59], v35 offset:3072\n\t"
    "v_mfma_f32_16x16x32_bf16 v[104:107], a[156:159], v[72:75], v[104:107]\n\t"
    "ds_read_b128 v[60:63], v35 offset:3136\n\t"
    "v_mfma_f32_16x16x32_bf16 v[104:107], a[168:171], v[76:79], v[104:107]\n\t"
    "ds_read_b128 v[64:67], v35 offset:3200\n\t"
    "v_mfma_f32_16x16x32_bf16 v[108:111], a[148:151], v[68:71], v[108:111]\n\t"
    "ds_read_b64_tr_b16 a[188:189], v33 offset:0\n\t"
    "v_mfma_f32_16x16x32_bf16 v[108:111], a[160:163], v[72:75], v[108:111]\n\t"
    "ds_read_b64_tr_b16 a[190:191], v33 offset:768\n\t"
    "v_mfma_f32_16x16x32_bf16 v[108:111], a[172:175], v[76:79], v[108:111]\n\t"
    "ds_read_b64_tr_b16 a[200:201], v33 offset:3072\n\t"
    "v_mfma_f32_16x16x32_bf16 v[112:115], a[152:155], v[68:71], v[112:115]\n\t"
    "ds_read_b64_tr_b16 a[202:203], v33 offset:3840\n\t"
    "v_mfma_f32_16x16x32_bf16 v[112:115], a[164:167], v[72:75], v[112:115]\n\t"
    "ds_read_b64_tr_b16 a[212:213], v33 offset:6144\n\t"
    "v_mfma_f32_16x16x32_bf16 v[112:115], a[176:179], v[76:79], v[112:115]\n\t"
    "ds_read_b64_tr_b16 a[214:215], v33 offset:6912\n\t"
    "; ---- unit 24: row 1 j 2 b 0 ds [1]\n\t"
    "s_waitcnt lgkmcnt(15)\n\t"
    "v_mfma_f32_16x16x32_bf16 v[80:83], a[180:183], v[44:47], v[80:83]\n\t"
    "ds_read_b128 v[68:71], v35 offset:6144\n\t"
    "v_mfma_f32_16x16x32_bf16 v[80:83], a[192:195], v[48:51], v[80:83]\n\t"
    "v_mfma_f32_16x16x32_bf16 v[80:83], a[204:207], v[52:55], v[80:83]\n\t"
    "s_waitcnt lgkmcnt(10)\n\t"
    "v_mfma_f32_16x16x32_bf16 v[84:87], a[184:187], v[44:47], v[84:87]\n\t"
    "ds_read_b128 v[72:75], v35 offset:6208\n\t"
    "v_mfma_f32_16x16x32_bf16 v[84:87], a[196:199], v[48:51], v[84:87]\n\t"
    "v_mfma_f32_16x16x32_bf16 v[84:87], a[208:211], v[52:55], v[84:87]\n\t"
    "s_waitcnt lgkmcnt(2)\n\t"
    "v_mfma_f32_16x16x32_bf16 v[88:91], a[188:191], v[44:47], v[88:91]\n\t"
    "ds_read_b128 v[76:79], v35 offset:6272\n\t"
    "v_mfma_f32_16x16x32_bf16 v[88:91], a[200:203], v[48:51], v[88:91]\n\t"
    "v_mfma_f32_16x16x32_bf16 v[88:91], a[212:215], v[52:55], v[88:91]\n\t"
    "; ---- unit 25: row 1 j 2 b 1 ds [1]\n\t"
    "s_waitcnt vmcnt(5)\n\t"
    "s_barrier\n\t"
    "v_mfma_f32_16x16x32_bf16 v[92:95], a[180:183], v[56:59], v[92:95]\n\t"
    "v_add_u32 v36, s47, v11\n\t"
    "v_mfma_f32_16x16x32_bf16 v[92:95], a[192:195], v[60:63], v[92:95]\n\t"
    "s_mov_b32 s40, 129024\n\t"
    "v_mfma_f32_16x16x32_bf16 v[92:95], a[204:207], v[64:67], v[92:95]\n\t"
    "ds_read_b128 v[44:47], v36 offset:0\n\t"
    "v_mfma_f32_16x16x32_bf16 v[96:99], a[184:187], v[56:59], v[96:99]\n\t"
    "s_add_u32 m0, %[m0w], 102400\n\t"
    "v_mfma_f32_16x16x32_bf16 v[96:99], a[196:199], v[60:63], v[96:99]\n\t"
    "ds_read_b128 v[48:51], v36 offset:64\n\t"
    "v_mfma_f32_16x16x32_bf16 v[96:99], a[208:211], v[64:67], v[96:99]\n\t"
    "buffer_load_dwordx4 %[vw0], %[srdw], s40 offen lds\n\t"
    "v_mfma_f32_16x16x32_bf16 v[100:103], a[188:191], v[56:59], v[100:103]\n\t"
    "ds_read_b128 v[52:55], v36 offset:128\n\t"
    "v_mfma_f32_16x16x32_bf16 v[100:103], a[200:203], v[60:63], v[100:103]\n\t"
    "buffer_load_dwordx4 %[vw1], %[srdw], s40 offen offset:1024 lds\n\t"
    "v_mfma_f32_16x16x32_bf16 v[100:103], a[212:215], v[64:67], v[100:103]\n\t"
    "s_mov_b32 s40, 130560\n\t"
    "; ---- unit 26: row 1 j 2 b 2 ds [1]\n\t"
    "s_waitcnt lgkmcnt(3)\n\t"
    "v_mfma_f32_16x16x32_bf16 v[104:107], a[180:183], v[68:71], v[104:107]\n\t"
    "ds_read_b128 v[56:59], v36 offset:3072\n\t"
    "v_mfma_f32_16x16x32_bf16 v[104:107], a[192:195], v[72:75], v[104:107]\n\t"
    "buffer_load_dwordx4 %[vw0], %[srdw], s40 offen offset:2048 lds\n\t"
    "v_mfma_f32_16x16x32_bf16 v[104:107], a[204:207], v[76:79], v[104:107]\n\t"
    "s_mov_b32 s40, 132096\n\t"
    "v_mfma_f32_16x16x32_bf16 v[108:111], a[184:187], v[68:71], v[108:111]\n\t"
    "ds_read_b128 v[60:63], v36 offset:3136\n\t"
    "v_mfma_f32_16x16x32_bf16 v[108:111], a[196:199], v[72:75], v[108:111]\n\t"
    "buffer_load_dwordx4 %[vw0], %[srdw], s40 offen offset:3072 lds\n\t"
    "v_mfma_f32_16x16x32_bf16 v[108:111], a[208:211], v[76:79], v[108:111]\n\t"
    "s_mov_b32 s40, 136192\n\t"
    "v_mfma_f32_16x16x32_bf16 v[112:115], a[188:191], v[68:71], v[112:115]\n\t"
    "ds_read_b128 v[64:67], v36 offset:3200\n\t"
    "v_mfma_f32_16x16x32_bf16 v[112:115], a[200:203], v[72:75], v[112:115]\n\t"
    "s_add_u32 m0, %[m0w], 106496\n\t"
    "v_mfma_f32_16x16x32_bf16 v[112:115], a[212:215], v[76:79], v[112:115]\n\t"
    "s_mov_b32 exec_hi, 0\n\t"
    "buffer_load_dwordx4 %[vw0], %[srdw], s40 offen lds\n\t"
    "s_mov_b32 exec_hi, -1\n\t"
    "; ---- unit 27: row 2 j 0 b 0 ds [1, 0]\n\t"
    "s_waitcnt lgkmcnt(3)\n\t"
    "v_mfma_f32_16x16x32_bf16 v[116:119], a[108:111], v[44:47], v[116:119]\n\t"
    "ds_read_b128 v[68:71], v36 offset:6144\n\t"
    "v_mfma_f32_16x16x32_bf16 v[116:119], a[120:123], v[48:51], v[116:119]\n\t"
    "s_lshl_b32 s41, %[nh], 7\n\t"
    "v_mfma_f32_16x16x32_bf16 v[116:119], a[132:135], v[52:55], v[116:119]\n\t"
    "v_add_u32 v22, s41, v10\n\t"
    "v_mfma_f32_16x16x32_bf16 v[120:123], a[112:115], v[44:47], v[120:123]\n\t"
    "v_lshrrev_b32 v20, 3, v22\n\t"
    "v_mfma_f32_16x16x32_bf16 v[120:123], a[124:127], v[48:51], v[120:123]\n\t"
    "v_and_b32 v20, 0x30, v20\n\t"
    "v_mfma_f32_16x16x32_bf16 v[120:123], a[136:139], v[52:55], v[120:123]\n\t"
    "v_xor_b32 v20, v20, v22\n\t"
    "v_mfma_f32_16x16x32_bf16 v[124:127], a[116:119], v[44:47], v[124:127]\n\t"
    "ds_read_b128 v[72:75], v36 offset:6208\n\t"
    "v_mfma_f32_16x16x32_bf16 v[124:127], a[128:131], v[48:51], v[124:127]\n\t"
    "v_add_u32 v20, %[sbase], v20\n\t"
    "v_mfma_f32_16x16x32_bf16 v[124:127], a[140:143], v[52:55], v[124:127]\n\t"
    "v_add_u32 v21, 64, v10\n\t"
    "v_mfma_f32_16x16x32_bf16 v[152:155], a[0:3], v[44:47], 0\n\t"
    "v_lshrrev_b32 v19, 3, v21\n\t"
    "v_mfma_f32_16x16x32_bf16 v[152:155], a[12:15], v[48:51], v[152:155]\n\t"
    "v_and_b32 v19, 0x30, v19\n\t"
    "v_mfma_f32_16x16x32_bf16 v[152:155], a[24:27], v[52:55], v[152:155]\n\t"
    "v_xor_b32 v19, v19, v21\n\t"
    "v_mfma_f32_16x16x32_bf16 v[156:159], a[4:7], v[44:47], 0\n\t"
    "ds_read_b128 v[76:79], v36 offset:6272\n\t"
    "v_mfma_f32_16x16x32_bf16 v[156:159], a[16:19], v[48:51], v[156:159]\n\t"
    "s_lshl_b32 s41, %[nh], 3\n\t"
    "v_mfma_f32_16x16x32_bf16 v[156:159], a[28:31], v[52:55], v[156:159]\n\t"
    "v_add_u32 v21, s41, v21\n\t"
    "v_mfma_f32_16x16x32_bf16 v[160:163], a[8:11], v[44:47], 0\n\t"
    "s_add_u32 s41, s41, %[sbase]\n\t"
    "v_mfma_f32_16x16x32_bf16 v[160:163], a[20:23], v[48:51], v[160:163]\n\t"
    "v_add_u32 v19, s41, v19\n\t"
    "v_mfma_f32_16x16x32_bf16 v[160:163], a[32:35], v[52:55], v[160:163]\n\t"
    "s_mul_i32 s41, %[nh], 4608\n\t"
    "; ---- unit 28: row 2 j 0 b 1 ds [1, 0]\n\t"
    "s_waitcnt lgkmcnt(3)\n\t"
    "v_mfma_f32_16x16x32_bf16 v[128:131], a[108:111], v[56:59], v[128:131]\n\t"
    "v_add_u32 v37, s47, v12\n\t"
    "v_mfma_f32_16x16x32_bf16 v[128:131], a[120:123], v[60:63], v[128:131]\n\t"
    "v_add_u32 v17, s41, %[vl16]\n\t"
    "v_mfma_f32_16x16x32_bf16 v[128:131], a[132:135], v[64:67], v[128:131]\n\t"
    "v_lshrrev_b32 v18, 3, %[vl16]\n\t"
    "v_mfma_f32_16x16x32_bf16 v[132:135], a[112:115], v[56:59], v[132:135]\n\t"
    "v_and_b32 v18, 0x30, v18\n\t"
    "v_mfma_f32_16x16x32_bf16 v[132:135], a[124:127], v[60:63], v[132:135]\n\t"
    "ds_read_b128 v[44:47], v37 offset:0\n\t"
    "v_mfma_f32_16x16x32_bf16 v[132:135], a[136:139], v[64:67], v[132:135]\n\t"
    "v_xor_b32 v18, v18, v17\n\t"
    "v_mfma_f32_16x16x32_bf16 v[136:139], a[116:119], v[56:59], v[136:139]\n\t"
    "v_add_u32 v18, %[sbase], v18\n\t"
    "v_mfma_f32_16x16x32_bf16 v[136:139], a[128:131], v[60:63], v[136:139]\n\t"
    "v_add_u32 v16, 0x1000, v17\n\t"
    "v_mfma_f32_16x16x32_bf16 v[136:139], a[140:143], v[64:67], v[136:139]\n\t"
    "v_lshlrev_b32 v23, 22, %[vl16]\n\t"
    "v_mfma_f32_16x16x32_bf16 v[164:167], a[0:3], v[56:59], 0\n\t"
    "ds_read_b128 v[48:51], v37 offset:64\n\t"
    "v_mfma_f32_16x16x32_bf16 v[164:167], a[12:15], v[60:63], v[164:167]\n\t"
    "v_and_b32 v23, 0x80000000, v23\n\t"
    "v_mfma_f32_16x16x32_bf16 v[164:167], a[24:27], v[64:67], v[164:167]\n\t"
    "v_or_b32 v16, v16, v23\n\t"
    "v_mfma_f32_16x16x32_bf16 v[168:171], a[4:7], v[56:59], 0\n\t"
    "v_mfma_f32_16x16x32_bf16 v[168:171], a[16:19], v[60:63], v[168:171]\n\t"
    "ds_read_b128 v[52:55], v37 offset:128\n\t"
    "v_mfma_f32_16x16x32_bf16 v[168:171], a[28:31], v[64:67], v[168:171]\n\t"
    "v_mfma_f32_16x16x32_bf16 v[172:175], a[8:11], v[56:59], 0\n\t"
    "v_mfma_f32_16x16x32_bf16 v[172:175], a[20:23], v[60:63], v[172:175]\n\t"
    "v_mfma_f32_16x16x32_bf16 v[172:175], a[32:35], v[64:67], v[172:175]\n\t"
    "; ---- unit 29: row 2 j 0 b 2 ds [1, 0]\n\t"
    "s_waitcnt lgkmcnt(3)\n\t"
    "v_mfma_f32_16x16x32_bf16 v[140:143], a[108:111], v[68:71], v[140:143]\n\t"
    "ds_read_b128 v[56:59], v37 offset:3072\n\t"
    "v_mfma_f32_16x16x32_bf16 v[140:143], a[120:123], v[72:75], v[140:143]\n\t"
    "s_mov_b32 s40, 147456\n\t"
    "v_mfma_f32_16x16x32_bf16 v[140:143], a[132:135], v[76:79], v[140:143]\n\t"
    "s_add_u32 m0, %[m0w], 122880\n\t"
    "v_mfma_f32_16x16x32_bf16 v[144:147], a[112:115], v[68:71], v[144:147]\n\t"
    "buffer_load_dwordx4 %[vw0], %[srdw], s40 offen lds\n\t"
    "v_mfma_f32_16x16x32_bf16 v[144:147], a[124:127], v[72:75], v[144:147]\n\t"
    "buffer_load_dwordx4 %[vw1], %[srdw], s40 offen offset:1024 lds\n\t"
    "v_mfma_f32_16x16x32_bf16 v[144:147], a[136:139], v[76:79], v[144:147]\n\t"
    "s_mov_b32 s40, 148992\n\t"
    "v_mfma_f32_16x16x32_bf16 v[148:151], a[116:119], v[68:71], v[148:151]\n\t"
    "ds_read_b128 v[60:63], v37 offset:3136\n\t"
    "v_mfma_f32_16x16x32_bf16 v[148:151], a[128:131], v[72:75], v[148:151]\n\t"
    "buffer_load_dwordx4 %[vw0], %[srdw], s40 offen offset:2048 lds\n\t"
    "v_mfma_f32_16x16x32_bf16 v[148:151], a[140:143], v[76:79], v[148:151]\n\t"
    "s_mov_b32 s40, 150528\n\t"
    "v_mfma_f32_16x16x32_bf16 v[176:179], a[0:3], v[68:71], 0\n\t"
    "buffer_load_dwordx4 %[vw0], %[srdw], s40 offen offset:3072 lds\n\t"
    "v_mfma_f32_16x16x32_bf16 v[176:179], a[12:15], v[72:75], v[176:179]\n\t"
    "s_mov_b32 s40, 154624\n\t"
    "v_mfma_f32_16x16x32_bf16 v[176:179], a[24:27], v[76:79], v[176:179]\n\t"
    "s_add_u32 m0, %[m0w], 126976\n\t"
    "v_mfma_f32_16x16x32_bf16 v[180:183], a[4:7], v[68:71], 0\n\t"
    "ds_read_b128 v[64:67], v37 offset:3200\n\t"
    "v_mfma_f32_16x16x32_bf16 v[180:183], a[16:19], v[72:75], v[180:183]\n\t"
    "s_mov_b32 exec_hi, 0\n\t"
    "buffer_load_dwordx4 %[vw0], %[srdw], s40 offen lds\n\t"
    "s_mov_b32 exec_hi, -1\n\t"
    "v_mfma_f32_16x16x32_bf16 v[180:183], a[28:31], v[76:79], v[180:183]\n\t"
    "v_mfma_f32_16x16x32_bf16 v[184:187], a[8:11], v[68:71], 0\n\t"
    "v_mfma_f32_16x16x32_bf16 v[184:187], a[20:23], v[72:75], v[184:187]\n\t"
    "v_mfma_f32_16x16x32_bf16 v[184:187], a[32:35], v[76:79], v[184:187]\n\t"
    "; ---- unit 30: row 2 j 1 b 0 ds [1, 0]\n\t"
    "s_waitcnt vmcnt(10)\n\t"
    "s_barrier\n\t"
    "s_waitcnt lgkmcnt(3)\n\t"
    "v_mfma_f32_16x16x32_bf16 v[116:119], a[144:147], v[44:47], v[116:119]\n\t"
    "ds_read_b128 v[68:71], v37 offset:6144\n\t"
    "v_mfma_f32_16x16x32_bf16 v[116:119], a[156:159], v[48:51], v[116:119]\n\t"
    "ds_read_b128 v[72:75], v37 offset:6208\n\t"
    "v_mfma_f32_16x16x32_bf16 v[116:119], a[168:171], v[52:55], v[116:119]\n\t"
    "ds_read_b128 v[76:79], v37 offset:6272\n\t"
    "v_mfma_f32_16x16x32_bf16 v[120:123], a[148:151], v[44:47], v[120:123]\n\t"
    "v_mfma_f32_16x16x32_bf16 v[120:123], a[160:163], v[48:51], v[120:123]\n\t"
    "v_add_u32 v32, 81920, v15\n\t"
    "v_mfma_f32_16x16x32_bf16 v[120:123], a[172:175], v[52:55], v[120:123]\n\t"
    "v_add_u32 v33, 81920, v14\n\t"
    "v_mfma_f32_16x16x32_bf16 v[124:127], a[152:155], v[44:47], v[124:127]\n\t"
    "ds_read_b64_tr_b16 a[216:217], v32 offset:0\n\t"
    "v_mfma_f32_16x16x32_bf16 v[124:127], a[164:167], v[48:51], v[124:127]\n\t"
    "v_mfma_f32_16x16x32_bf16 v[124:127], a[176:179], v[52:55], v[124:127]\n\t"
    "ds_read_b64_tr_b16 a[218:219], v32 offset:768\n\t"
    "v_mfma_f32_16x16x32_bf16 v[152:155], a[36:39], v[44:47], v[152:155]\n\t"
    "ds_read_b64_tr_b16 a[228:229], v32 offset:3072\n\t"
    "v_mfma_f32_16x16x32_bf16 v[152:155], a[48:51], v[48:51], v[152:155]\n\t"
    "v_mfma_f32_16x16x32_bf16 v[152:155], a[60:63], v[52:55], v[152:155]\n\t"
    "ds_read_b64_tr_b16 a[230:231], v32 offset:3840\n\t"
    "v_mfma_f32_16x16x32_bf16 v[156:159], a[40:43], v[44:47], v[156:159]\n\t"
    "ds_read_b64_tr_b16 a[240:241], v32 offset:6144\n\t"
    "v_mfma_f32_16x16x32_bf16 v[156:159], a[52:55], v[48:51], v[156:159]\n\t"
    "ds_read_b64_tr_b16 a[242:243], v32 offset:6912\n\t"
    "v_mfma_f32_16x16x32_bf16 v[156:159], a[64:67], v[52:55], v[156:159]\n\t"
    "v_mfma_f32_16x16x32_bf16 v[160:163], a[44:47], v[44:47], v[160:163]\n\t"
    "ds_read_b64_tr_b16 a[220:221], v32 offset:8\n\t"
    "v_mfma_f32_16x16x32_bf16 v[160:163], a[56:59], v[48:51], v[160:163]\n\t"
    "ds_read_b64_tr_b16 a[222:223], v32 offset:776\n\t"
    "v_mfma_f32_16x16x32_bf16 v[160:163], a[68:71], v[52:55], v[160:163]\n\t"
    "; ---- unit 31: row 2 j 1 b 1 ds [1, 0]\n\t"
    "s_waitcnt lgkmcnt(11)\n\t"
    "v_mfma_f32_16x16x32_bf16 v[128:131], a[144:147], v[56:59], v[128:131]\n\t"
    "v_add_u32 v34, s47, v13\n\t"
    "v_mfma_f32_16x16x32_bf16 v[128:131], a[156:159], v[60:63], v[128:131]\n\t"
    "ds_read_b128 v[44:47], v34 offset:0\n\t"
    "v_mfma_f32_16x16x32_bf16 v[128:131], a[168:171], v[64:67], v[128:131]\n\t"
    "ds_read_b128 v[48:51], v34 offset:64\n\t"
    "v_mfma_f32_16x16x32_bf16 v[132:135], a[148:151], v[56:59], v[132:135]\n\t"
    "ds_read_b128 v[52:55], v34 offset:128\n\t"
    "v_mfma_f32_16x16x32_bf16 v[132:135], a[160:163], v[60:63], v[132:135]\n\t"
    "v_mfma_f32_16x16x32_bf16 v[132:135], a[172:175], v[64:67], v[132:135]\n\t"
    "ds_read_b64_tr_b16 a[232:233], v32 offset:3080\n\t"
    "v_mfma_f32_16x16x32_bf16 v[136:139], a[152:155], v[56:59], v[136:139]\n\t"
    "ds_read_b64_tr_b16 a[234:235], v32 offset:3848\n\t"
    "v_mfma_f32_16x16x32_bf16 v[136:139], a[164:167], v[60:63], v[136:139]\n\t"
    "ds_read_b64_tr_b16 a[244:245], v32 offset:6152\n\t"
    "v_mfma_f32_16x16x32_bf16 v[136:139], a[176:179], v[64:67], v[136:139]\n\t"
    "v_mfma_f32_16x16x32_bf16 v[164:167], a[36:39], v[56:59], v[164:167]\n\t"
    "ds_read_b64_tr_b16 a[246:247], v32 offset:6920\n\t"
    "v_mfma_f32_16x16x32_bf16 v[164:167], a[48:51], v[60:63], v[164:167]\n\t"
    "ds_read_b64_tr_b16 a[224:225], v33 offset:0\n\t"
    "v_mfma_f32_16x16x32_bf16 v[164:167], a[60:63], v[64:67], v[164:167]\n\t"
    "ds_read_b64_tr_b16 a[226:227], v33 offset:768\n\t"
    "v_mfma_f32_16x16x32_bf16 v[168:171], a[40:43], v[56:59], v[168:171]\n\t"
    "ds_read_b64_tr_b16 a[236:237], v33 offset:3072\n\t"
    "v_mfma_f32_16x16x32_bf16 v[168:171], a[52:55], v[60:63], v[168:171]\n\t"
    "v_mfma_f32_16x16x32_bf16 v[168:171], a[64:67], v[64:67], v[168:171]\n\t"
    "ds_read_b64_tr_b16 a[238:239], v33 offset:3840\n\t"
    "v_mfma_f32_16x16x32_bf16 v[172:175], a[44:47], v[56:59], v[172:175]\n\t"
    "ds_read_b64_tr_b16 a[248:249], v33 offset:6144\n\t"
    "v_mfma_f32_16x16x32_bf16 v[172:175], a[56:59], v[60:63], v[172:175]\n\t"
    "ds_read_b64_tr_b16 a[250:251], v33 offset:6912\n\t"
    "v_mfma_f32_16x16x32_bf16 v[172:175], a[68:71], v[64:67], v[172:175]\n\t"
    "; ---- unit 32: row 2 j 1 b 2 ds [1, 0]\n\t"
    "s_waitcnt lgkmcnt(15)\n\t"
    "v_mfma_f32_16x16x32_bf16 v[140:143], a[144:147], v[68:71], v[140:143]\n\t"
    "ds_read_b128 v[56:59], v34 offset:3072\n\t"
    "v_mfma_f32_16x16x32_bf16 v[140:143], a[156:159], v[72:75], v[140:143]\n\t"
    "v_mfma_f32_16x16x32_bf16 v[140:143], a[168:171], v[76:79], v[140:143]\n\t"
    "v_mfma_f32_16x16x32_bf16 v[144:147], a[148:151], v[68:71], v[144:147]\n\t"
    "v_mfma_f32_16x16x32_bf16 v[144:147], a[160:163], v[72:75], v[144:147]\n\t"
    "v_mfma_f32_16x16x32_bf16 v[144:147], a[172:175], v[76:79], v[144:147]\n\t"
    "v_mfma_f32_16x16x32_bf16 v[148:151], a[152:155], v[68:71], v[148:151]\n\t"
    "ds_read_b128 v[60:63], v34 offset:3136\n\t"
    "v_mfma_f32_16x16x32_bf16 v[148:151], a[164:167], v[72:75], v[148:151]\n\t"
    "v_mfma_f32_16x16x32_bf16 v[148:151], a[176:179], v[76:79], v[148:151]\n\t"
    "v_mfma_f32_16x16x32_bf16 v[176:179], a[36:39], v[68:71], v[176:179]\n\t"
    "v_mfma_f32_16x16x32_bf16 v[176:179], a[48:51], v[72:75], v[176:179]\n\t"
    "v_mfma_f32_16x16x32_bf16 v[176:179], a[60:63], v[76:79], v[176:179]\n\t"
    "v_mfma_f32_16x16x32_bf16 v[180:183], a[40:43], v[68:71], v[180:183]\n\t"
    "ds_read_b128 v[64:67], v34 offset:3200\n\t"
    "v_mfma_f32_16x16x32_bf16 v[180:183], a[52:55], v[72:75], v[180:183]\n\t"
    "v_mfma_f32_16x16x32_bf16 v[180:183], a[64:67], v[76:79], v[180:183]\n\t"
    "v_mfma_f32_16x16x32_bf16 v[184:187], a[44:47], v[68:71], v[184:187]\n\t"
    "v_mfma_f32_16x16x32_bf16 v[184:187], a[56:59], v[72:75], v[184:187]\n\t"
    "v_mfma_f32_16x16x32_bf16 v[184:187], a[68:71], v[76:79], v[184:187]\n\t"
    "; ---- unit 33: row 2 j 2 b 0 ds [1, 0]\n\t"
    "s_waitcnt vmcnt(5) lgkmcnt(3)\n\t"
    "s_barrier\n\t"
    "v_mfma_f32_16x16x32_bf16 v[116:119], a[180:183], v[44:47], v[116:119]\n\t"
    "ds_read_b128 v[68:71], v34 offset:6144\n\t"
    "v_mfma_f32_16x16x32_bf16 v[116:119], a[192:195], v[48:51], v[116:119]\n\t"
    "ds_read_b128 v[72:75], v34 offset:6208\n\t"
    "v_mfma_f32_16x16x32_bf16 v[116:119], a[204:207], v[52:55], v[116:119]\n\t"
    "ds_read_b128 v[76:79], v34 offset:6272\n\t"
    "v_mfma_f32_16x16x32_bf16 v[120:123], a[184:187], v[44:47], v[120:123]\n\t"
    "s_add_u32 s44, %[xso], 442368\n\t"
    "v_mfma_f32_16x16x32_bf16 v[120:123], a[196:199], v[48:51], v[120:123]\n\t"
    "v_add_u32 v32, 102400, v15\n\t"
    "v_mfma_f32_16x16x32_bf16 v[120:123], a[208:211], v[52:55], v[120:123]\n\t"
    "v_add_u32 v33, 102400, v14\n\t"
    "v_mfma_f32_16x16x32_bf16 v[124:127], a[188:191], v[44:47], v[124:127]\n\t"
    "ds_read_b64_tr_b16 a[252:253], v32 offset:0\n\t"
    "v_mfma_f32_16x16x32_bf16 v[124:127], a[200:203], v[48:51], v[124:127]\n\t"
    "s_add_u32 m0, %[m0x], 143360\n\t"
    "v_mfma_f32_16x16x32_bf16 v[124:127], a[212:215], v[52:55], v[124:127]\n\t"
    "ds_read_b64_tr_b16 a[254:255], v32 offset:768\n\t"
    "v_mfma_f32_16x16x32_bf16 v[152:155], a[72:75], v[44:47], v[152:155]\n\t"
    "ds_read_b64_tr_b16 v[196:197], v32 offset:3072\n\t"
    "v_mfma_f32_16x16x32_bf16 v[152:155], a[84:87], v[48:51], v[152:155]\n\t"
    "buffer_load_dwordx4 %[vx0], %[srdx], s44 offen offset:0 lds\n\t"
    "v_mfma_f32_16x16x32_bf16 v[152:155], a[96:99], v[52:55], v[152:155]\n\t"
    "ds_read_b64_tr_b16 v[198:199], v32 offset:3840\n\t"
    "v_mfma_f32_16x16x32_bf16 v[156:159], a[76:79], v[44:47], v[156:159]\n\t"
    "ds_read_b64_tr_b16 v[208:209], v32 offset:6144\n\t"
    "v_mfma_f32_16x16x32_bf16 v[156:159], a[88:91], v[48:51], v[156:159]\n\t"
    "ds_read_b64_tr_b16 v[210:211], v32 offset:6912\n\t"
    "v_mfma_f32_16x16x32_bf16 v[156:159], a[100:103], v[52:55], v[156:159]\n\t"
    "buffer_load_dwordx4 %[vx1], %[srdx], s44 offen offset:1024 lds\n\t"
    "v_mfma_f32_16x16x32_bf16 v[160:163], a[80:83], v[44:47], v[160:163]\n\t"
    "ds_read_b64_tr_b16 v[188:189], v32 offset:8\n\t"
    "v_mfma_f32_16x16x32_bf16 v[160:163], a[92:95], v[48:51], v[160:163]\n\t"
    "ds_read_b64_tr_b16 v[190:191], v32 offset:776\n\t"
    "v_mfma_f32_16x16x32_bf16 v[160:163], a[104:107], v[52:55], v[160:163]\n\t"
    "buffer_load_dwordx4 %[vx2], %[srdx], s44 offen offset:2048 lds\n\t"
    "; ---- unit 34: row 2 j 2 b 1 ds [1, 0]\n\t"
    "s_waitcnt lgkmcnt(11)\n\t"
    "v_mfma_f32_16x16x32_bf16 v[128:131], a[180:183], v[56:59], v[128:131]\n\t"
    "ds_read_b128 v[44:47], v36 offset:0\n\t"
    "v_mfma_f32_16x16x32_bf16 v[128:131], a[192:195], v[60:63], v[128:131]\n\t"
    "ds_read_b128 v[48:51], v36 offset:64\n\t"
    "v_mfma_f32_16x16x32_bf16 v[128:131], a[204:207], v[64:67], v[128:131]\n\t"
    "ds_read_b128 v[52:55], v36 offset:128\n\t"
    "v_mfma_f32_16x16x32_bf16 v[132:135], a[184:187], v[56:59], v[132:135]\n\t"
    "buffer_load_dwordx4 %[vx3], %[srdx], s44 offen offset:3072 lds\n\t"
    "v_mfma_f32_16x16x32_bf16 v[132:135], a[196:199], v[60:63], v[132:135]\n\t"
    "ds_read_b64_tr_b16 v[200:201], v32 offset:3080\n\t"
    "v_mfma_f32_16x16x32_bf16 v[132:135], a[208:211], v[64:67], v[132:135]\n\t"
    "ds_read_b64_tr_b16 v[202:203], v32 offset:3848\n\t"
    "v_mfma_f32_16x16x32_bf16 v[136:139], a[188:191], v[56:59], v[136:139]\n\t"
    "ds_read_b64_tr_b16 v[212:213], v32 offset:6152\n\t"
    "v_mfma_f32_16x16x32_bf16 v[136:139], a[200:203], v[60:63], v[136:139]\n\t"
    "s_add_u32 m0, %[m0x], 147456\n\t"
    "v_mfma_f32_16x16x32_bf16 v[136:139], a[212:215], v[64:67], v[136:139]\n\t"
    "ds_read_b64_tr_b16 v[214:215], v32 offset:6920\n\t"
    "v_mfma_f32_16x16x32_bf16 v[164:167], a[72:75], v[56:59], v[164:167]\n\t"
    "ds_read_b64_tr_b16 v[192:193], v33 offset:0\n\t"
    "v_mfma_f32_16x16x32_bf16 v[164:167], a[84:87], v[60:63], v[164:167]\n\t"
    "buffer_load_dwordx4 %[vx4], %[srdx], s44 offen lds\n\t"
    "v_mfma_f32_16x16x32_bf16 v[164:167], a[96:99], v[64:67], v[164:167]\n\t"
    "ds_read_b64_tr_b16 v[194:195], v33 offset:768\n\t"
    "v_mfma_f32_16x16x32_bf16 v[168:171], a[76:79], v[56:59], v[168:171]\n\t"
    "ds_read_b64_tr_b16 v[204:205], v33 offset:3072\n\t"
    "v_mfma_f32_16x16x32_bf16 v[168:171], a[88:91], v[60:63], v[168:171]\n\t"
    "ds_read_b64_tr_b16 v[206:207], v33 offset:3840\n\t"
    "v_mfma_f32_16x16x32_bf16 v[168:171], a[100:103], v[64:67], v[168:171]\n\t"
    "v_mfma_f32_16x16x32_bf16 v[172:175], a[80:83], v[56:59], v[172:175]\n\t"
    "ds_read_b64_tr_b16 v[216:217], v33 offset:6144\n\t"
    "v_mfma_f32_16x16x32_bf16 v[172:175], a[92:95], v[60:63], v[172:175]\n\t"
    "ds_read_b64_tr_b16 v[218:219], v33 offset:6912\n\t"
    "v_mfma_f32_16x16x32_bf16 v[172:175], a[104:107], v[64:67], v[172:175]\n\t"
    "; ---- unit 35: row 2 j 2 b 2 ds [1, 0]\n\t"
    "s_waitcnt lgkmcnt(15)\n\t"
    "v_mfma_f32_16x16x32_bf16 v[140:143], a[180:183], v[68:71], v[140:143]\n\t"
    "ds_read_b128 v[56:59], v36 offset:3072\n\t"
    "v_mfma_f32_16x16x32_bf16 v[140:143], a[192:195], v[72:75], v[140:143]\n\t"
    "v_mfma_f32_16x16x32_bf16 v[140:143], a[204:207], v[76:79], v[140:143]\n\t"
    "v_mfma_f32_16x16x32_bf16 v[144:147], a[184:187], v[68:71], v[144:147]\n\t"
    "v_mfma_f32_16x16x32_bf16 v[144:147], a[196:199], v[72:75], v[144:147]\n\t"
    "v_mfma_f32_16x16x32_bf16 v[144:147], a[208:211], v[76:79], v[144:147]\n\t"
    "v_mfma_f32_16x16x32_bf16 v[148:151], a[188:191], v[68:71], v[148:151]\n\t"
    "ds_read_b128 v[60:63], v36 offset:3136\n\t"
    "v_mfma_f32_16x16x32_bf16 v[148:151], a[200:203], v[72:75], v[148:151]\n\t"
    "v_mfma_f32_16x16x32_bf16 v[148:151], a[212:215], v[76:79], v[148:151]\n\t"
    "v_mfma_f32_16x16x32_bf16 v[176:179], a[72:75], v[68:71], v[176:179]\n\t"
    "v_mfma_f32_16x16x32_bf16 v[176:179], a[84:87], v[72:75], v[176:179]\n\t"
    "v_mfma_f32_16x16x32_bf16 v[176:179], a[96:99], v[76:79], v[176:179]\n\t"
    "v_mfma_f32_16x16x32_bf16 v[180:183], a[76:79], v[68:71], v[180:183]\n\t"
    "ds_read_b128 v[64:67], v36 offset:3200\n\t"
    "v_mfma_f32_16x16x32_bf16 v[180:183], a[88:91], v[72:75], v[180:183]\n\t"
    "v_mfma_f32_16x16x32_bf16 v[180:183], a[100:103], v[76:79], v[180:183]\n\t"
    "v_mfma_f32_16x16x32_bf16 v[184:187], a[80:83], v[68:71], v[184:187]\n\t"
    "v_mfma_f32_16x16x32_bf16 v[184:187], a[92:95], v[72:75], v[184:187]\n\t"
    "v_mfma_f32_16x16x32_bf16 v[184:187], a[104:107], v[76:79], v[184:187]\n\t"
    "; ---- unit 36: row 2 j 0 b 0 ds [2]\n\t"
    "s_waitcnt lgkmcnt(13)\n\t"
    "v_mfma_f32_16x16x32_bf16 v[80:83], a[216:219], v[44:47], v[80:83]\n\t"
    "ds_read_b128 v[68:71], v36 offset:6144\n\t"
    "v_mfma_f32_16x16x32_bf16 v[80:83], a[228:231], v[48:51], v[80:83]\n\t"
    "v_mfma_f32_16x16x32_bf16 v[80:83], a[240:243], v[52:55], v[80:83]\n\t"
    "v_mfma_f32_16x16x32_bf16 v[84:87], a[220:223], v[44:47], v[84:87]\n\t"
    "ds_read_b128 v[72:75], v36 offset:6208\n\t"
    "v_mfma_f32_16x16x32_bf16 v[84:87], a[232:235], v[48:51], v[84:87]\n\t"
    "v_mfma_f32_16x16x32_bf16 v[84:87], a[244:247], v[52:55], v[84:87]\n\t"
    "v_mfma_f32_16x16x32_bf16 v[88:91], a[224:227], v[44:47], v[88:91]\n\t"
    "ds_read_b128 v[76:79], v36 offset:6272\n\t"
    "v_mfma_f32_16x16x32_bf16 v[88:91], a[236:239], v[48:51], v[88:91]\n\t"
    "v_mfma_f32_16x16x32_bf16 v[88:91], a[248:251], v[52:55], v[88:91]\n\t"
    "; ---- unit 37: row 2 j 0 b 1 ds [2]\n\t"
    "s_waitcnt vmcnt(5) lgkmcnt(6)\n\t"
    "s_barrier\n\t"
    "s_waitcnt lgkmcnt(3)\n\t"
    "v_mfma_f32_16x16x32_bf16 v[92:95], a[216:219], v[56:59], v[92:95]\n\t"
    "ds_read_b128 v[44:47], v37 offset:0\n\t"
    "ds_read_b128 v[48:51], v37 offset:64\n\t"
    "v_mfma_f32_16x16x32_bf16 v[92:95], a[228:231], v[60:63], v[92:95]\n\t"
    "ds_read_b128 v[52:55], v37 offset:128\n\t"
    "v_mfma_f32_16x16x32_bf16 v[92:95], a[240:243], v[64:67], v[92:95]\n\t"
    "v_add_u32 v32, 122880, v15\n\t"
    "v_mfma_f32_16x16x32_bf16 v[96:99], a[220:223], v[56:59], v[96:99]\n\t"
    "v_add_u32 v33, 122880, v14\n\t"
    "v_mfma_f32_16x16x32_bf16 v[96:99], a[232:235], v[60:63], v[96:99]\n\t"
    "ds_read_b64_tr_b16 v[220:221], v32 offset:0\n\t"
    "v_mfma_f32_16x16x32_bf16 v[96:99], a[244:247], v[64:67], v[96:99]\n\t"
    "ds_read_b64_tr_b16 v[222:223], v32 offset:768\n\t"
    "v_mfma_f32_16x16x32_bf16 v[100:103], a[224:227], v[56:59], v[100:103]\n\t"
    "ds_read_b64_tr_b16 v[232:233], v32 offset:3072\n\t"
    "v_mfma_f32_16x16x32_bf16 v[100:103], a[236:239], v[60:63], v[100:103]\n\t"
    "ds_read_b64_tr_b16 v[234:235], v32 offset:3840\n\t"
    "v_mfma_f32_16x16x32_bf16 v[100:103], a[248:251], v[64:67], v[100:103]\n\t"
    "ds_read_b64_tr_b16 v[244:245], v32 offset:6144\n\t"
    "; ---- unit 38: row 2 j 0 b 2 ds [2]\n\t"
    "s_waitcnt lgkmcnt(8)\n\t"
    "v_mfma_f32_16x16x32_bf16 v[104:107], a[216:219], v[68:71], v[104:107]\n\t"
    "ds_read_b128 v[56:59], v37 offset:3072\n\t"
    "ds_read_b128 v[60:63], v37 offset:3136\n\t"
    "s_add_u32 s44, %[xso], 589824\n\t"
    "v_mfma_f32_16x16x32_bf16 v[104:107], a[228:231], v[72:75], v[104:107]\n\t"
    "ds_read_b128 v[64:67], v37 offset:3200\n\t"
    "s_add_u32 m0, %[m0x], 61440\n\t"
    "v_mfma_f32_16x16x32_bf16 v[104:107], a[240:243], v[76:79], v[104:107]\n\t"
    "ds_read_b64_tr_b16 v[246:247], v32 offset:6912\n\t"
    "buffer_load_dwordx4 %[vx0], %[srdx], s44 offen offset:0 lds\n\t"
    "v_mfma_f32_16x16x32_bf16 v[108:111], a[220:223], v[68:71], v[108:111]\n\t"
    "ds_read_b64_tr_b16 v[224:225], v32 offset:8\n\t"
    "buffer_load_dwordx4 %[vx1], %[srdx], s44 offen offset:1024 lds\n\t"
    "v_mfma_f32_16x16x32_bf16 v[108:111], a[232:235], v[72:75], v[108:111]\n\t"
    "ds_read_b64_tr_b16 v[226:227], v32 offset:776\n\t"
    "buffer_load_dwordx4 %[vx2], %[srdx], s44 offen offset:2048 lds\n\t"
    "v_mfma_f32_16x16x32_bf16 v[108:111], a[244:247], v[76:79], v[108:111]\n\t"
    "ds_read_b64_tr_b16 v[236:237], v32 offset:3080\n\t"
    "buffer_load_dwordx4 %[vx3], %[srdx], s44 offen offset:3072 lds\n\t"
    "v_mfma_f32_16x16x32_bf16 v[112:115], a[224:227], v[68:71], v[112:115]\n\t"
    "ds_read_b64_tr_b16 v[238:239], v32 offset:3848\n\t"
    "s_add_u32 m0, %[m0x], 65536\n\t"
    "v_mfma_f32_16x16x32_bf16 v[112:115], a[236:239], v[72:75], v[112:115]\n\t"
    "ds_read_b64_tr_b16 v[248:249], v32 offset:6152\n\t"
    "buffer_load_dwordx4 %[vx4], %[srdx], s44 offen lds\n\t"
    "v_mfma_f32_16x16x32_bf16 v[112:115], a[248:251], v[76:79], v[112:115]\n\t"
    "ds_read_b64_tr_b16 v[250:251], v32 offset:6920\n\t"
    "; ---- unit 39: row 2 j 1 b 0 ds [2]\n\t"
    "s_waitcnt lgkmcnt(15)\n\t"
    "v_mfma_f32_16x16x32_bf16 v[80:83], a[252:255], v[44:47], v[80:83]\n\t"
    "ds_read_b128 v[68:71], v37 offset:6144\n\t"
    "v_mfma_f32_16x16x32_bf16 v[80:83], v[196:199], v[48:51], v[80:83]\n\t"
    "ds_read_b128 v[72:75], v37 offset:6208\n\t"
    "v_mfma_f32_16x16x32_bf16 v[80:83], v[208:211], v[52:55], v[80:83]\n\t"
    "ds_read_b128 v[76:79], v37 offset:6272\n\t"
    "v_mfma_f32_16x16x32_bf16 v[84:87], v[188:191], v[44:47], v[84:87]\n\t"
    "ds_read_b64_tr_b16 v[228:229], v33 offset:0\n\t"
    "v_mfma_f32_16x16x32_bf16 v[84:87], v[200:203], v[48:51], v[84:87]\n\t"
    "ds_read_b64_tr_b16 v[230:231], v33 offset:768\n\t"
    "v_mfma_f32_16x16x32_bf16 v[84:87], v[212:215], v[52:55], v[84:87]\n\t"
    "ds_read_b64_tr_b16 v[240:241], v33 offset:3072\n\t"
    "v_mfma_f32_16x16x32_bf16 v[88:91], v[192:195], v[44:47], v[88:91]\n\t"
    "ds_read_b64_tr_b16 v[242:243], v33 offset:3840\n\t"
    "v_mfma_f32_16x16x32_bf16 v[88:91], v[204:207], v[48:51], v[88:91]\n\t"
    "ds_read_b64_tr_b16 v[252:253], v33 offset:6144\n\t"
    "v_mfma_f32_16x16x32_bf16 v[88:91], v[216:219], v[52:55], v[88:91]\n\t"
    "ds_read_b64_tr_b16 v[254:255], v33 offset:6912\n\t"
    "; ---- unit 40: row 2 j 1 b 1 ds [2]\n\t"
    "s_waitcnt lgkmcnt(15)\n\t"
    "v_mfma_f32_16x16x32_bf16 v[92:95], a[252:255], v[56:59], v[92:95]\n\t"
    "ds_read_b128 v[44:47], v34 offset:0\n\t"
    "v_mfma_f32_16x16x32_bf16 v[92:95], v[196:199], v[60:63], v[92:95]\n\t"
    "v_mfma_f32_16x16x32_bf16 v[92:95], v[208:211], v[64:67], v[92:95]\n\t"
    "v_mfma_f32_16x16x32_bf16 v[96:99], v[188:191], v[56:59], v[96:99]\n\t"
    "ds_read_b128 v[48:51], v34 offset:64\n\t"
    "v_mfma_f32_16x16x32_bf16 v[96:99], v[200:203], v[60:63], v[96:99]\n\t"
    "v_mfma_f32_16x16x32_bf16 v[96:99], v[212:215], v[64:67], v[96:99]\n\t"
    "v_mfma_f32_16x16x32_bf16 v[100:103], v[192:195], v[56:59], v[100:103]\n\t"
    "ds_read_b128 v[52:55], v34 offset:128\n\t"
    "v_mfma_f32_16x16x32_bf16 v[100:103], v[204:207], v[60:63], v[100:103]\n\t"
    "v_mfma_f32_16x16x32_bf16 v[100:103], v[216:219], v[64:67], v[100:103]\n\t"
    "; ---- unit 41: row 2 j 1 b 2 ds [2]\n\t"
    "s_waitcnt vmcnt(5) lgkmcnt(3)\n\t"
    "s_barrier\n\t"
    "v_mfma_f32_16x16x32_bf16 v[104:107], a[252:255], v[68:71], v[104:107]\n\t"
    "ds_read_b128 v[56:59], v34 offset:3072\n\t"
    "v_mfma_f32_16x16x32_bf16 v[104:107], v[196:199], v[72:75], v[104:107]\n\t"
    "v_mfma_f32_16x16x32_bf16 v[104:107], v[208:211], v[76:79], v[104:107]\n\t"
    "v_mfma_f32_16x16x32_bf16 v[108:111], v[188:191], v[68:71], v[108:111]\n\t"
    "ds_read_b128 v[60:63], v34 offset:3136\n\t"
    "v_mfma_f32_16x16x32_bf16 v[108:111], v[200:203], v[72:75], v[108:111]\n\t"
    "v_mfma_f32_16x16x32_bf16 v[108:111], v[212:215], v[76:79], v[108:111]\n\t"
    "v_mfma_f32_16x16x32_bf16 v[112:115], v[192:195], v[68:71], v[112:115]\n\t"
    "ds_read_b128 v[64:67], v34 offset:3200\n\t"
    "v_mfma_f32_16x16x32_bf16 v[112:115], v[204:207], v[72:75], v[112:115]\n\t"
    "v_mfma_f32_16x16x32_bf16 v[112:115], v[216:219], v[76:79], v[112:115]\n\t"
    "; ---- unit 42: row 2 j 2 b 0 ds [2]\n\t"
    "s_waitcnt lgkmcnt(3)\n\t"
    "v_mfma_f32_16x16x32_bf16 v[80:83], v[220:223], v[44:47], v[80:83]\n\t"
    "ds_read_b128 v[68:71], v34 offset:6144\n\t"
    "v_mfma_f32_16x16x32_bf16 v[80:83], v[232:235], v[48:51], v[80:83]\n\t"
    "v_mfma_f32_16x16x32_bf16 v[80:83], v[244:247], v[52:55], v[80:83]\n\t"
    "v_mfma_f32_16x16x32_bf16 v[84:87], v[224:227], v[44:47], v[84:87]\n\t"
    "ds_read_b128 v[72:75], v34 offset:6208\n\t"
    "v_mfma_f32_16x16x32_bf16 v[84:87], v[236:239], v[48:51], v[84:87]\n\t"
    "v_mfma_f32_16x16x32_bf16 v[84:87], v[248:251], v[52:55], v[84:87]\n\t"
    "v_mfma_f32_16x16x32_bf16 v[88:91], v[228:231], v[44:47], v[88:91]\n\t"
    "ds_read_b128 v[76:79], v34 offset:6272\n\t"
    "v_mfma_f32_16x16x32_bf16 v[88:91], v[240:243], v[48:51], v[88:91]\n\t"
    "v_mfma_f32_16x16x32_bf16 v[88:91], v[252:255], v[52:55], v[88:91]\n\t"
    "; ---- unit 43: row 2 j 2 b 1 ds [2]\n\t"
    "s_waitcnt lgkmcnt(3)\n\t"
    "v_mfma_f32_16x16x32_bf16 v[92:95], v[220:223], v[56:59], v[92:95]\n\t"
    "v_add_u32 v35, s48, v11\n\t"
    "v_mfma_f32_16x16x32_bf16 v[92:95], v[232:235], v[60:63], v[92:95]\n\t"
    "v_add_u32 v32, 122880, v20\n\t"
    "v_mfma_f32_16x16x32_bf16 v[92:95], v[244:247], v[64:67], v[92:95]\n\t"
    "ds_read_b128 v[44:47], v35 offset:0\n\t"
    "v_mfma_f32_16x16x32_bf16 v[96:99], v[224:227], v[56:59], v[96:99]\n\t"
    "v_add_u32 v33, 122880, v19\n\t"
    "v_mfma_f32_16x16x32_bf16 v[96:99], v[236:239], v[60:63], v[96:99]\n\t"
    "ds_read_b128 v[48:51], v35 offset:64\n\t"
    "v_mfma_f32_16x16x32_bf16 v[96:99], v[248:251], v[64:67], v[96:99]\n\t"
    "v_cvt_pk_bf16_f32 v38, v80, v81\n\t"
    "v_mfma_f32_16x16x32_bf16 v[100:103], v[228:231], v[56:59], v[100:103]\n\t"
    "ds_read_b128 v[52:55], v35 offset:128\n\t"
    "v_mfma_f32_16x16x32_bf16 v[100:103], v[240:243], v[60:63], v[100:103]\n\t"
    "v_cvt_pk_bf16_f32 v39, v82, v83\n\t"
    "v_mfma_f32_16x16x32_bf16 v[100:103], v[252:255], v[64:67], v[100:103]\n\t"
    "v_cvt_pk_bf16_f32 v40, v84, v85\n\t"
    "; ---- unit 44: row 2 j 2 b 2 ds [2]\n\t"
    "s_waitcnt lgkmcnt(3)\n\t"
    "v_mfma_f32_16x16x32_bf16 v[104:107], v[220:223], v[68:71], v[104:107]\n\t"
    "ds_read_b128 v[56:59], v35 offset:3072\n\t"
    "v_mfma_f32_16x16x32_bf16 v[104:107], v[232:235], v[72:75], v[104:107]\n\t"
    "v_cvt_pk_bf16_f32 v41, v86, v87\n\t"
    "v_mfma_f32_16x16x32_bf16 v[104:107], v[244:247], v[76:79], v[104:107]\n\t"
    "ds_write_b128 v32, v[38:41] offset:0\n\t"
    "v_mfma_f32_16x16x32_bf16 v[108:111], v[224:227], v[68:71], v[108:111]\n\t"
    "ds_read_b128 v[60:63], v35 offset:3136\n\t"
    "v_mfma_f32_16x16x32_bf16 v[108:111], v[236:239], v[72:75], v[108:111]\n\t"
    "v_cvt_pk_bf16_f32 v42, v88, v89\n\t"
    "v_mfma_f32_16x16x32_bf16 v[108:111], v[248:251], v[76:79], v[108:111]\n\t"
    "v_cvt_pk_bf16_f32 v43, v90, v91\n\t"
    "v_mfma_f32_16x16x32_bf16 v[112:115], v[228:231], v[68:71], v[112:115]\n\t"
    "ds_read_b128 v[64:67], v35 offset:3200\n\t"
    "v_mfma_f32_16x16x32_bf16 v[112:115], v[240:243], v[72:75], v[112:115]\n\t"
    "ds_write_b64 v33, v[42:43] offset:0\n\t"
    "v_mfma_f32_16x16x32_bf16 v[112:115], v[252:255], v[76:79], v[112:115]\n\t"
    "s_nop 1\n\t"
    "v_cvt_pk_bf16_f32 v38, v92, v93\n\t"
    "; ---- unit 45: row 3 j 0 b 0 ds [2, 1, 0]\n\t"
    "s_waitcnt lgkmcnt(5)\n\t"
    "v_mfma_f32_16x16x32_bf16 v[116:119], a[216:219], v[44:47], v[116:119]\n\t"
    "ds_read_b128 v[68:71], v35 offset:6144\n\t"
    "v_mfma_f32_16x16x32_bf16 v[116:119], a[228:231], v[48:51], v[116:119]\n\t"
    "v_cvt_pk_bf16_f32 v39, v94, v95\n\t"
    "v_mfma_f32_16x16x32_bf16 v[116:119], a[240:243], v[52:55], v[116:119]\n\t"
    "v_cvt_pk_bf16_f32 v40, v96, v97\n\t"
    "v_mfma_f32_16x16x32_bf16 v[120:123], a[220:223], v[44:47], v[120:123]\n\t"
    "v_cvt_pk_bf16_f32 v41, v98, v99\n\t"
    "v_mfma_f32_16x16x32_bf16 v[120:123], a[232:235], v[48:51], v[120:123]\n\t"
    "ds_write_b128 v32, v[38:41] offset:3072\n\t"
    "v_mfma_f32_16x16x32_bf16 v[120:123], a[244:247], v[52:55], v[120:123]\n\t"
    "s_nop 1\n\t"
    "v_cvt_pk_bf16_f32 v42, v100, v101\n\t"
    "v_mfma_f32_16x16x32_bf16 v[124:127], a[224:227], v[44:47], v[124:127]\n\t"
    "v_cvt_pk_bf16_f32 v43, v102, v103\n\t"
    "v_mfma_f32_16x16x32_bf16 v[124:127], a[236:239], v[48:51], v[124:127]\n\t"
    "ds_write_b64 v33, v[42:43] offset:3072\n\t"
    "v_mfma_f32_16x16x32_bf16 v[124:127], a[248:251], v[52:55], v[124:127]\n\t"
    "s_nop 1\n\t"
    "v_cvt_pk_bf16_f32 v38, v104, v105\n\t"
    "v_mfma_f32_16x16x32_bf16 v[152:155], a[108:111], v[44:47], v[152:155]\n\t"
    "ds_read_b128 v[72:75], v35 offset:6208\n\t"
    "v_mfma_f32_16x16x32_bf16 v[152:155], a[120:123], v[48:51], v[152:155]\n\t"
    "v_cvt_pk_bf16_f32 v39, v106, v107\n\t"
    "v_mfma_f32_16x16x32_bf16 v[152:155], a[132:135], v[52:55], v[152:155]\n\t"
    "v_cvt_pk_bf16_f32 v40, v108, v109\n\t"
    "v_mfma_f32_16x16x32_bf16 v[156:159], a[112:115], v[44:47], v[156:159]\n\t"
    "v_cvt_pk_bf16_f32 v41, v110, v111\n\t"
    "v_mfma_f32_16x16x32_bf16 v[156:159], a[124:127], v[48:51], v[156:159]\n\t"
    "ds_write_b128 v32, v[38:41] offset:6144\n\t"
    "v_mfma_f32_16x16x32_bf16 v[156:159], a[136:139], v[52:55], v[156:159]\n\t"
    "s_nop 1\n\t"
    "v_cvt_pk_bf16_f32 v42, v112, v113\n\t"
    "v_mfma_f32_16x16x32_bf16 v[160:163], a[116:119], v[44:47], v[160:163]\n\t"
    "v_cvt_pk_bf16_f32 v43, v114, v115\n\t"
    "v_mfma_f32_16x16x32_bf16 v[160:163], a[128:131], v[48:51], v[160:163]\n\t"
    "ds_write_b64 v33, v[42:43] offset:6144\n\t"
    "v_mfma_f32_16x16x32_bf16 v[160:163], a[140:143], v[52:55], v[160:163]\n\t"
    "v_mfma_f32_16x16x32_bf16 v[80:83], a[0:3], v[44:47], 0\n\t"
    "ds_read_b128 v[76:79], v35 offset:6272\n\t"
    "v_mfma_f32_16x16x32_bf16 v[80:83], a[12:15], v[48:51], v[80:83]\n\t"
    "v_mfma_f32_16x16x32_bf16 v[80:83], a[24:27], v[52:55], v[80:83]\n\t"
    "v_mfma_f32_16x16x32_bf16 v[84:87], a[4:7], v[44:47], 0\n\t"
    "v_mfma_f32_16x16x32_bf16 v[84:87], a[16:19], v[48:51], v[84:87]\n\t"
    "v_mfma_f32_16x16x32_bf16 v[84:87], a[28:31], v[52:55], v[84:87]\n\t"
    "v_mfma_f32_16x16x32_bf16 v[88:91], a[8:11], v[44:47], 0\n\t"
    "v_mfma_f32_16x16x32_bf16 v[88:91], a[20:23], v[48:51], v[88:91]\n\t"
    "v_mfma_f32_16x16x32_bf16 v[88:91], a[32:35], v[52:55], v[88:91]\n\t"
    "; ---- unit 46: row 3 j 0 b 1 ds [2, 1, 0]\n\t"
    "s_waitcnt lgkmcnt(8)\n\t"
    "v_mfma_f32_16x16x32_bf16 v[128:131], a[216:219], v[56:59], v[128:131]\n\t"
    "v_add_u32 v36, s48, v12\n\t"
    "v_mfma_f32_16x16x32_bf16 v[128:131], a[228:231], v[60:63], v[128:131]\n\t"
    "v_mfma_f32_16x16x32_bf16 v[128:131], a[240:243], v[64:67], v[128:131]\n\t"
    "v_mfma_f32_16x16x32_bf16 v[132:135], a[220:223], v[56:59], v[132:135]\n\t"
    "v_mfma_f32_16x16x32_bf16 v[132:135], a[232:235], v[60:63], v[132:135]\n\t"
    "v_mfma_f32_16x16x32_bf16 v[132:135], a[244:247], v[64:67], v[132:135]\n\t"
    "v_mfma_f32_16x16x32_bf16 v[136:139], a[224:227], v[56:59], v[136:139]\n\t"
    "ds_read_b128 v[44:47], v36 offset:0\n\t"
    "v_mfma_f32_16x16x32_bf16 v[136:139], a[236:239], v[60:63], v[136:139]\n\t"
    "v_mfma_f32_16x16x32_bf16 v[136:139], a[248:251], v[64:67], v[136:139]\n\t"
    "v_mfma_f32_16x16x32_bf16 v[164:167], a[108:111], v[56:59], v[164:167]\n\t"
    "v_mfma_f32_16x16x32_bf16 v[164:167], a[120:123], v[60:63], v[164:167]\n\t"
    "v_mfma_f32_16x16x32_bf16 v[164:167], a[132:135], v[64:67], v[164:167]\n\t"
    "v_mfma_f32_16x16x32_bf16 v[168:171], a[112:115], v[56:59], v[168:171]\n\t"
    "v_mfma_f32_16x16x32_bf16 v[168:171], a[124:127], v[60:63], v[168:171]\n\t"
    "ds_read_b128 v[48:51], v36 offset:64\n\t"
    "v_mfma_f32_16x16x32_bf16 v[168:171], a[136:139], v[64:67], v[168:171]\n\t"
    "v_mfma_f32_16x16x32_bf16 v[172:175], a[116:119], v[56:59], v[172:175]\n\t"
    "v_mfma_f32_16x16x32_bf16 v[172:175], a[128:131], v[60:63], v[172:175]\n\t"
    "v_mfma_f32_16x16x32_bf16 v[172:175], a[140:143], v[64:67], v[172:175]\n\t"
    "v_mfma_f32_16x16x32_bf16 v[92:95], a[0:3], v[56:59], 0\n\t"
    "v_mfma_f32_16x16x32_bf16 v[92:95], a[12:15], v[60:63], v[92:95]\n\t"
    "v_mfma_f32_16x16x32_bf16 v[92:95], a[24:27], v[64:67], v[92:95]\n\t"
    "ds_read_b128 v[52:55], v36 offset:128\n\t"
    "v_mfma_f32_16x16x32_bf16 v[96:99], a[4:7], v[56:59], 0\n\t"
    "v_mfma_f32_16x16x32_bf16 v[96:99], a[16:19], v[60:63], v[96:99]\n\t"
    "v_mfma_f32_16x16x32_bf16 v[96:99], a[28:31], v[64:67], v[96:99]\n\t"
    "v_mfma_f32_16x16x32_bf16 v[100:103], a[8:11], v[56:59], 0\n\t"
    "v_mfma_f32_16x16x32_bf16 v[100:103], a[20:23], v[60:63], v[100:103]\n\t"
    "v_mfma_f32_16x16x32_bf16 v[100:103], a[32:35], v[64:67], v[100:103]\n\t"
    "; ---- unit 47: row 3 j 0 b 2 ds [2, 1, 0]\n\t"
    "s_waitcnt vmcnt(0) lgkmcnt(4)\n\t"
    "s_barrier\n\t"
    "s_waitcnt lgkmcnt(3)\n\t"
    "v_mfma_f32_16x16x32_bf16 v[140:143], a[216:219], v[68:71], v[140:143]\n\t"
    "ds_read_b128 v[56:59], v36 offset:3072\n\t"
    "v_mfma_f32_16x16x32_bf16 v[140:143], a[228:231], v[72:75], v[140:143]\n\t"
    "v_add_u32 v23, 122880, v18\n\t"
    "v_mfma_f32_16x16x32_bf16 v[140:143], a[240:243], v[76:79], v[140:143]\n\t"
    "s_add_u32 s42, %[oso], 0\n\t"
    "v_mfma_f32_16x16x32_bf16 v[144:147], a[220:223], v[68:71], v[144:147]\n\t"
    "ds_read_b128 v[24:27], v23 offset:0\n\t"
    "v_mfma_f32_16x16x32_bf16 v[144:147], a[232:235], v[72:75], v[144:147]\n\t"
    "ds_read_b128 v[28:31], v23 offset:1024\n\t"
    "v_mfma_f32_16x16x32_bf16 v[144:147], a[244:247], v[76:79], v[144:147]\n\t"
    "v_mfma_f32_16x16x32_bf16 v[148:151], a[224:227], v[68:71], v[148:151]\n\t"
    "v_mfma_f32_16x16x32_bf16 v[148:151], a[236:239], v[72:75], v[148:151]\n\t"
    "v_mfma_f32_16x16x32_bf16 v[148:151], a[248:251], v[76:79], v[148:151]\n\t"
    "v_mfma_f32_16x16x32_bf16 v[176:179], a[108:111], v[68:71], v[176:179]\n\t"
    "ds_read_b128 v[60:63], v36 offset:3136\n\t"
    "v_mfma_f32_16x16x32_bf16 v[176:179], a[120:123], v[72:75], v[176:179]\n\t"
    "s_waitcnt lgkmcnt(2)\n\t"
    "buffer_store_dwordx4 v[24:27], v17, %[srdo], s42 offen offset:0 sc1\n\t"
    "v_mfma_f32_16x16x32_bf16 v[176:179], a[132:135], v[76:79], v[176:179]\n\t"
    "ds_read_b128 v[24:27], v23 offset:2048\n\t"
    "v_mfma_f32_16x16x32_bf16 v[180:183], a[112:115], v[68:71], v[180:183]\n\t"
    "s_waitcnt lgkmcnt(2)\n\t"
    "buffer_store_dwordx4 v[28:31], v17, %[srdo], s42 offen offset:1024 sc1\n\t"
    "v_mfma_f32_16x16x32_bf16 v[180:183], a[124:127], v[72:75], v[180:183]\n\t"
    "ds_read_b128 v[28:31], v23 offset:3072\n\t"
    "v_mfma_f32_16x16x32_bf16 v[180:183], a[136:139], v[76:79], v[180:183]\n\t"
    "v_mfma_f32_16x16x32_bf16 v[184:187], a[116:119], v[68:71], v[184:187]\n\t"
    "v_mfma_f32_16x16x32_bf16 v[184:187], a[128:131], v[72:75], v[184:187]\n\t"
    "v_mfma_f32_16x16x32_bf16 v[184:187], a[140:143], v[76:79], v[184:187]\n\t"
    "v_mfma_f32_16x16x32_bf16 v[104:107], a[0:3], v[68:71], 0\n\t"
    "ds_read_b128 v[64:67], v36 offset:3200\n\t"
    "v_mfma_f32_16x16x32_bf16 v[104:107], a[12:15], v[72:75], v[104:107]\n\t"
    "s_waitcnt lgkmcnt(2)\n\t"
    "buffer_store_dwordx4 v[24:27], v17, %[srdo], s42 offen offset:2048 sc1\n\t"
    "v_mfma_f32_16x16x32_bf16 v[104:107], a[24:27], v[76:79], v[104:107]\n\t"
    "ds_read_b128 v[24:27], v23 offset:4096\n\t"
    "v_mfma_f32_16x16x32_bf16 v[108:111], a[4:7], v[68:71], 0\n\t"
    "s_waitcnt lgkmcnt(2)\n\t"
    "buffer_store_dwordx4 v[28:31], v17, %[srdo], s42 offen offset:3072 sc1\n\t"
    "v_mfma_f32_16x16x32_bf16 v[108:111], a[16:19], v[72:75], v[108:111]\n\t"
    "v_mfma_f32_16x16x32_bf16 v[108:111], a[28:31], v[76:79], v[108:111]\n\t"
    "v_mfma_f32_16x16x32_bf16 v[112:115], a[8:11], v[68:71], 0\n\t"
    "v_mfma_f32_16x16x32_bf16 v[112:115], a[20:23], v[72:75], v[112:115]\n\t"
    "v_mfma_f32_16x16x32_bf16 v[112:115], a[32:35], v[76:79], v[112:115]\n\t"
    "; ---- unit 48: row 3 j 1 b 0 ds [2, 1, 0]\n\t"
    "v_mfma_f32_16x16x32_bf16 v[116:119], a[252:255], v[44:47], v[116:119]\n\t"
    "ds_read_b128 v[68:71], v36 offset:6144\n\t"
    "v_mfma_f32_16x16x32_bf16 v[116:119], v[196:199], v[48:51], v[116:119]\n\t"
    "s_waitcnt lgkmcnt(1)\n\t"
    "buffer_store_dwordx4 v[24:27], v16, %[srdo], s42 offen sc1\n\t"
    "v_mfma_f32_16x16x32_bf16 v[116:119], v[208:211], v[52:55], v[116:119]\n\t"
    "s_add_u32 s44, %[xso], 737280\n\t"
    "v_mfma_f32_16x16x32_bf16 v[120:123], v[188:191], v[44:47], v[120:123]\n\t"
    "s_add_u32 m0, %[m0x], 81920\n\t"
    "v_mfma_f32_16x16x32_bf16 v[120:123], v[200:203], v[48:51], v[120:123]\n\t"
    "buffer_load_dwordx4 %[vx0], %[srdx], s44 offen offset:0 lds\n\t"
    "v_mfma_f32_16x16x32_bf16 v[120:123], v[212:215], v[52:55], v[120:123]\n\t"
    "buffer_load_dwordx4 %[vx1], %[srdx], s44 offen offset:1024 lds\n\t"
    "v_mfma_f32_16x16x32_bf16 v[124:127], v[192:195], v[44:47], v[124:127]\n\t"
    "buffer_load_dwordx4 %[vx2], %[srdx], s44 offen offset:2048 lds\n\t"
    "v_mfma_f32_16x16x32_bf16 v[124:127], v[204:207], v[48:51], v[124:127]\n\t"
    "buffer_load_dwordx4 %[vx3], %[srdx], s44 offen offset:3072 lds\n\t"
    "v_mfma_f32_16x16x32_bf16 v[124:127], v[216:219], v[52:55], v[124:127]\n\t"
    "s_add_u32 m0, %[m0x], 86016\n\t"
    "v_mfma_f32_16x16x32_bf16 v[152:155], a[144:147], v[44:47], v[152:155]\n\t"
    "ds_read_b128 v[72:75], v36 offset:6208\n\t"
    "v_mfma_f32_16x16x32_bf16 v[152:155], a[156:159], v[48:51], v[152:155]\n\t"
    "buffer_load_dwordx4 %[vx4], %[srdx], s44 offen lds\n\t"
    "v_mfma_f32_16x16x32_bf16 v[152:155], a[168:171], v[52:55], v[152:155]\n\t"
    "v_mfma_f32_16x16x32_bf16 v[156:159], a[148:151], v[44:47], v[156:159]\n\t"
    "v_mfma_f32_16x16x32_bf16 v[156:159], a[160:163], v[48:51], v[156:159]\n\t"
    "v_mfma_f32_16x16x32_bf16 v[156:159], a[172:175], v[52:55], v[156:159]\n\t"
    "v_mfma_f32_16x16x32_bf16 v[160:163], a[152:155], v[44:47], v[160:163]\n\t"
    "v_mfma_f32_16x16x32_bf16 v[160:163], a[164:167], v[48:51], v[160:163]\n\t"
    "v_mfma_f32_16x16x32_bf16 v[160:163], a[176:179], v[52:55], v[160:163]\n\t"
    "v_mfma_f32_16x16x32_bf16 v[80:83], a[36:39], v[44:47], v[80:83]\n\t"
    "ds_read_b128 v[76:79], v36 offset:6272\n\t"
    "v_mfma_f32_16x16x32_bf16 v[80:83], a[48:51], v[48:51], v[80:83]\n\t"
    "v_mfma_f32_16x16x32_bf16 v[80:83], a[60:63], v[52:55], v[80:83]\n\t"
    "v_mfma_f32_16x16x32_bf16 v[84:87], a[40:43], v[44:47], v[84:87]\n\t"
    "v_mfma_f32_16x16x32_bf16 v[84:87], a[52:55], v[48:51], v[84:87]\n\t"
    "v_mfma_f32_16x16x32_bf16 v[84:87], a[64:67], v[52:55], v[84:87]\n\t"
    "v_mfma_f32_16x16x32_bf16 v[88:91], a[44:47], v[44:47], v[88:91]\n\t"
    "v_mfma_f32_16x16x32_bf16 v[88:91], a[56:59], v[48:51], v[88:91]\n\t"
    "v_mfma_f32_16x16x32_bf16 v[88:91], a[68:71], v[52:55], v[88:91]\n\t"
    "; ---- unit 49: row 3 j 1 b 1 ds [2, 1, 0]\n\t"
    "v_mfma_f32_16x16x32_bf16 v[128:131], a[252:255], v[56:59], v[128:131]\n\t"
    "v_add_u32 v37, s48, v13\n\t"
    "v_mfma_f32_16x16x32_bf16 v[128:131], v[196:199], v[60:63], v[128:131]\n\t"
    "v_mfma_f32_16x16x32_bf16 v[128:131], v[208:211], v[64:67], v[128:131]\n\t"
    "v_mfma_f32_16x16x32_bf16 v[132:135], v[188:191], v[56:59], v[132:135]\n\t"
    "v_mfma_f32_16x16x32_bf16 v[132:135], v[200:203], v[60:63], v[132:135]\n\t"
    "v_mfma_f32_16x16x32_bf16 v[132:135], v[212:215], v[64:67], v[132:135]\n\t"
    "v_mfma_f32_16x16x32_bf16 v[136:139], v[192:195], v[56:59], v[136:139]\n\t"
    "ds_read_b128 v[44:47], v37 offset:0\n\t"
    "v_mfma_f32_16x16x32_bf16 v[136:139], v[204:207], v[60:63], v[136:139]\n\t"
    "v_mfma_f32_16x16x32_bf16 v[136:139], v[216:219], v[64:67], v[136:139]\n\t"
    "v_mfma_f32_16x16x32_bf16 v[164:167], a[144:147], v[56:59], v[164:167]\n\t"
    "v_mfma_f32_16x16x32_bf16 v[164:167], a[156:159], v[60:63], v[164:167]\n\t"
    "v_mfma_f32_16x16x32_bf16 v[164:167], a[168:171], v[64:67], v[164:167]\n\t"
    "v_mfma_f32_16x16x32_bf16 v[168:171], a[148:151], v[56:59], v[168:171]\n\t"
    "v_mfma_f32_16x16x32_bf16 v[168:171], a[160:163], v[60:63], v[168:171]\n\t"
    "ds_read_b128 v[48:51], v37 offset:64\n\t"
    "v_mfma_f32_16x16x32_bf16 v[168:171], a[172:175], v[64:67], v[168:171]\n\t"
    "v_mfma_f32_16x16x32_bf16 v[172:175], a[152:155], v[56:59], v[172:175]\n\t"
    "v_mfma_f32_16x16x32_bf16 v[172:175], a[164:167], v[60:63], v[172:175]\n\t"
    "v_mfma_f32_16x16x32_bf16 v[172:175], a[176:179], v[64:67], v[172:175]\n\t"
    "v_mfma_f32_16x16x32_bf16 v[92:95], a[36:39], v[56:59], v[92:95]\n\t"
    "v_mfma_f32_16x16x32_bf16 v[92:95], a[48:51], v[60:63], v[92:95]\n\t"
    "v_mfma_f32_16x16x32_bf16 v[92:95], a[60:63], v[64:67], v[92:95]\n\t"
    "ds_read_b128 v[52:55], v37 offset:128\n\t"
    "v_mfma_f32_16x16x32_bf16 v[96:99], a[40:43], v[56:59], v[96:99]\n\t"
    "v_mfma_f32_16x16x32_bf16 v[96:99], a[52:55], v[60:63], v[96:99]\n\t"
    "v_mfma_f32_16x16x32_bf16 v[96:99], a[64:67], v[64:67], v[96:99]\n\t"
    "v_mfma_f32_16x16x32_bf16 v[100:103], a[44:47], v[56:59], v[100:103]\n\t"
    "v_mfma_f32_16x16x32_bf16 v[100:103], a[56:59], v[60:63], v[100:103]\n\t"
    "v_mfma_f32_16x16x32_bf16 v[100:103], a[68:71], v[64:67], v[100:103]\n\t"
    "; ---- unit 50: row 3 j 1 b 2 ds [2, 1, 0]\n\t"
    "s_waitcnt lgkmcnt(3)\n\t"
    "v_mfma_f32_16x16x32_bf16 v[140:143], a[252:255], v[68:71], v[140:143]\n\t"
    "ds_read_b128 v[56:59], v37 offset:3072\n\t"
    "v_mfma_f32_16x16x32_bf16 v[140:143], v[196:199], v[72:75], v[140:143]\n\t"
    "v_mfma_f32_16x16x32_bf16 v[140:143], v[208:211], v[76:79], v[140:143]\n\t"
    "v_mfma_f32_16x16x32_bf16 v[144:147], v[188:191], v[68:71], v[144:147]\n\t"
    "v_mfma_f32_16x16x32_bf16 v[144:147], v[200:203], v[72:75], v[144:147]\n\t"
    "v_mfma_f32_16x16x32_bf16 v[144:147], v[212:215], v[76:79], v[144:147]\n\t"
    "v_mfma_f32_16x16x32_bf16 v[148:151], v[192:195], v[68:71], v[148:151]\n\t"
    "v_mfma_f32_16x16x32_bf16 v[148:151], v[204:207], v[72:75], v[148:151]\n\t"
    "v_mfma_f32_16x16x32_bf16 v[148:151], v[216:219], v[76:79], v[148:151]\n\t"
    "v_mfma_f32_16x16x32_bf16 v[176:179], a[144:147], v[68:71], v[176:179]\n\t"
    "ds_read_b128 v[60:63], v37 offset:3136\n\t"
    "v_mfma_f32_16x16x32_bf16 v[176:179], a[156:159], v[72:75], v[176:179]\n\t"
    "v_mfma_f32_16x16x32_bf16 v[176:179], a[168:171], v[76:79], v[176:179]\n\t"
    "v_mfma_f32_16x16x32_bf16 v[180:183], a[148:151], v[68:71], v[180:183]\n\t"
    "v_mfma_f32_16x16x32_bf16 v[180:183], a[160:163], v[72:75], v[180:183]\n\t"
    "v_mfma_f32_16x16x32_bf16 v[180:183], a[172:175], v[76:79], v[180:183]\n\t"
    "v_mfma_f32_16x16x32_bf16 v[184:187], a[152:155], v[68:71], v[184:187]\n\t"
    "v_mfma_f32_16x16x32_bf16 v[184:187], a[164:167], v[72:75], v[184:187]\n\t"
    "v_mfma_f32_16x16x32_bf16 v[184:187], a[176:179], v[76:79], v[184:187]\n\t"
    "v_mfma_f32_16x16x32_bf16 v[104:107], a[36:39], v[68:71], v[104:107]\n\t"
    "ds_read_b128 v[64:67], v37 offset:3200\n\t"
    "v_mfma_f32_16x16x32_bf16 v[104:107], a[48:51], v[72:75], v[104:107]\n\t"
    "v_mfma_f32_16x16x32_bf16 v[104:107], a[60:63], v[76:79], v[104:107]\n\t"
    "v_mfma_f32_16x16x32_bf16 v[108:111], a[40:43], v[68:71], v[108:111]\n\t"
    "v_mfma_f32_16x16x32_bf16 v[108:111], a[52:55], v[72:75], v[108:111]\n\t"
    "v_mfma_f32_16x16x32_bf16 v[108:111], a[64:67], v[76:79], v[108:111]\n\t"
    "v_mfma_f32_16x16x32_bf16 v[112:115], a[44:47], v[68:71], v[112:115]\n\t"
    "v_mfma_f32_16x16x32_bf16 v[112:115], a[56:59], v[72:75], v[112:115]\n\t"
    "v_mfma_f32_16x16x32_bf16 v[112:115], a[68:71], v[76:79], v[112:115]\n\t"
    "; ---- unit 51: row 3 j 2 b 0 ds [2, 1, 0]\n\t"
    "s_waitcnt lgkmcnt(3)\n\t"
    "v_mfma_f32_16x16x32_bf16 v[116:119], v[220:223], v[44:47], v[116:119]\n\t"
    "ds_read_b128 v[68:71], v37 offset:6144\n\t"
    "v_mfma_f32_16x16x32_bf16 v[116:119], v[232:235], v[48:51], v[116:119]\n\t"
    "v_mfma_f32_16x16x32_bf16 v[116:119], v[244:247], v[52:55], v[116:119]\n\t"
    "v_mfma_f32_16x16x32_bf16 v[120:123], v[224:227], v[44:47], v[120:123]\n\t"
    "v_mfma_f32_16x16x32_bf16 v[120:123], v[236:239], v[48:51], v[120:123]\n\t"
    "v_mfma_f32_16x16x32_bf16 v[120:123], v[248:251], v[52:55], v[120:123]\n\t"
    "v_mfma_f32_16x16x32_bf16 v[124:127], v[228:231], v[44:47], v[124:127]\n\t"
    "v_mfma_f32_16x16x32_bf16 v[124:127], v[240:243], v[48:51], v[124:127]\n\t"
    "v_mfma_f32_16x16x32_bf16 v[124:127], v[252:255], v[52:55], v[124:127]\n\t"
    "v_mfma_f32_16x16x32_bf16 v[152:155], a[180:183], v[44:47], v[152:155]\n\t"
    "ds_read_b128 v[72:75], v37 offset:6208\n\t"
    "v_mfma_f32_16x16x32_bf16 v[152:155], a[192:195], v[48:51], v[152:155]\n\t"
    "v_add_u32 v32, 40960, v20\n\t"
    "v_mfma_f32_16x16x32_bf16 v[152:155], a[204:207], v[52:55], v[152:155]\n\t"
    "v_add_u32 v33, 40960, v19\n\t"
    "v_mfma_f32_16x16x32_bf16 v[156:159], a[184:187], v[44:47], v[156:159]\n\t"
    "v_cvt_pk_bf16_f32 v38, v116, v117\n\t"
    "v_mfma_f32_16x16x32_bf16 v[156:159], a[196:199], v[48:51], v[156:159]\n\t"
    "v_cvt_pk_bf16_f32 v39, v118, v119\n\t"
    "v_mfma_f32_16x16x32_bf16 v[156:159], a[208:211], v[52:55], v[156:159]\n\t"
    "v_cvt_pk_bf16_f32 v40, v120, v121\n\t"
    "v_mfma_f32_16x16x32_bf16 v[160:163], a[188:191], v[44:47], v[160:163]\n\t"
    "v_cvt_pk_bf16_f32 v41, v122, v123\n\t"
    "v_mfma_f32_16x16x32_bf16 v[160:163], a[200:203], v[48:51], v[160:163]\n\t"
    "ds_write_b128 v32, v[38:41] offset:0\n\t"
    "v_mfma_f32_16x16x32_bf16 v[160:163], a[212:215], v[52:55], v[160:163]\n\t"
    "s_nop 1\n\t"
    "v_cvt_pk_bf16_f32 v42, v124, v125\n\t"
    "v_mfma_f32_16x16x32_bf16 v[80:83], a[72:75], v[44:47], v[80:83]\n\t"
    "ds_read_b128 v[76:79], v37 offset:6272\n\t"
    "v_mfma_f32_16x16x32_bf16 v[80:83], a[84:87], v[48:51], v[80:83]\n\t"
    "v_cvt_pk_bf16_f32 v43, v126, v127\n\t"
    "v_mfma_f32_16x16x32_bf16 v[80:83], a[96:99], v[52:55], v[80:83]\n\t"
    "ds_write_b64 v33, v[42:43] offset:0\n\t"
    "v_mfma_f32_16x16x32_bf16 v[84:87], a[76:79], v[44:47], v[84:87]\n\t"
    "v_mfma_f32_16x16x32_bf16 v[84:87], a[88:91], v[48:51], v[84:87]\n\t"
    "v_mfma_f32_16x16x32_bf16 v[84:87], a[100:103], v[52:55], v[84:87]\n\t"
    "v_mfma_f32_16x16x32_bf16 v[88:91], a[80:83], v[44:47], v[88:91]\n\t"
    "v_mfma_f32_16x16x32_bf16 v[88:91], a[92:95], v[48:51], v[88:91]\n\t"
    "v_mfma_f32_16x16x32_bf16 v[88:91], a[104:107], v[52:55], v[88:91]\n\t"
    "; ---- unit 52: row 3 j 2 b 1 ds [2, 1, 0]\n\t"
    "s_waitcnt lgkmcnt(5)\n\t"
    "v_mfma_f32_16x16x32_bf16 v[128:131], v[220:223], v[56:59], v[128:131]\n\t"
    "v_add_u32 v34, s49, v11\n\t"
    "v_mfma_f32_16x16x32_bf16 v[128:131], v[232:235], v[60:63], v[128:131]\n\t"
    "v_mfma_f32_16x16x32_bf16 v[128:131], v[244:247], v[64:67], v[128:131]\n\t"
    "v_mfma_f32_16x16x32_bf16 v[132:135], v[224:227], v[56:59], v[132:135]\n\t"
    "v_mfma_f32_16x16x32_bf16 v[132:135], v[236:239], v[60:63], v[132:135]\n\t"
    "v_mfma_f32_16x16x32_bf16 v[132:135], v[248:251], v[64:67], v[132:135]\n\t"
    "v_mfma_f32_16x16x32_bf16 v[136:139], v[228:231], v[56:59], v[136:139]\n\t"
    "ds_read_b128 v[44:47], v34 offset:0\n\t"
    "v_mfma_f32_16x16x32_bf16 v[136:139], v[240:243], v[60:63], v[136:139]\n\t"
    "v_mfma_f32_16x16x32_bf16 v[136:139], v[252:255], v[64:67], v[136:139]\n\t"
    "v_mfma_f32_16x16x32_bf16 v[164:167], a[180:183], v[56:59], v[164:167]\n\t"
    "s_nop 1\n\t"
    "v_cvt_pk_bf16_f32 v38, v128, v129\n\t"
    "v_mfma_f32_16x16x32_bf16 v[164:167], a[192:195], v[60:63], v[164:167]\n\t"
    "v_cvt_pk_bf16_f32 v39, v130, v131\n\t"
    "v_mfma_f32_16x16x32_bf16 v[164:167], a[204:207], v[64:67], v[164:167]\n\t"
    "v_cvt_pk_bf16_f32 v40, v132, v133\n\t"
    "v_mfma_f32_16x16x32_bf16 v[168:171], a[184:187], v[56:59], v[168:171]\n\t"
    "v_cvt_pk_bf16_f32 v41, v134, v135\n\t"
    "v_mfma_f32_16x16x32_bf16 v[168:171], a[196:199], v[60:63], v[168:171]\n\t"
    "ds_read_b128 v[48:51], v34 offset:64\n\t"
    "v_mfma_f32_16x16x32_bf16 v[168:171], a[208:211], v[64:67], v[168:171]\n\t"
    "ds_write_b128 v32, v[38:41] offset:3072\n\t"
    "v_mfma_f32_16x16x32_bf16 v[172:175], a[188:191], v[56:59], v[172:175]\n\t"
    "s_nop 1\n\t"
    "v_cvt_pk_bf16_f32 v42, v136, v137\n\t"
    "v_mfma_f32_16x16x32_bf16 v[172:175], a[200:203], v[60:63], v[172:175]\n\t"
    "v_cvt_pk_bf16_f32 v43, v138, v139\n\t"
    "v_mfma_f32_16x16x32_bf16 v[172:175], a[212:215], v[64:67], v[172:175]\n\t"
    "ds_write_b64 v33, v[42:43] offset:3072\n\t"
    "v_mfma_f32_16x16x32_bf16 v[92:95], a[72:75], v[56:59], v[92:95]\n\t"
    "v_mfma_f32_16x16x32_bf16 v[92:95], a[84:87], v[60:63], v[92:95]\n\t"
    "v_mfma_f32_16x16x32_bf16 v[92:95], a[96:99], v[64:67], v[92:95]\n\t"
    "ds_read_b128 v[52:55], v34 offset:128\n\t"
    "v_mfma_f32_16x16x32_bf16 v[96:99], a[76:79], v[56:59], v[96:99]\n\t"
    "v_mfma_f32_16x16x32_bf16 v[96:99], a[88:91], v[60:63], v[96:99]\n\t"
    "v_mfma_f32_16x16x32_bf16 v[96:99], a[100:103], v[64:67], v[96:99]\n\t"
    "v_mfma_f32_16x16x32_bf16 v[100:103], a[80:83], v[56:59], v[100:103]\n\t"
    "v_mfma_f32_16x16x32_bf16 v[100:103], a[92:95], v[60:63], v[100:103]\n\t"
    "v_mfma_f32_16x16x32_bf16 v[100:103], a[104:107], v[64:67], v[100:103]\n\t"
    "; ---- unit 53: row 3 j 2 b 2 ds [2, 1, 0]\n\t"
    "s_waitcnt lgkmcnt(6)\n\t"
    "v_mfma_f32_16x16x32_bf16 v[140:143], v[220:223], v[68:71], v[140:143]\n\t"
    "ds_read_b128 v[56:59], v34 offset:3072\n\t"
    "v_mfma_f32_16x16x32_bf16 v[140:143], v[232:235], v[72:75], v[140:143]\n\t"
    "v_mfma_f32_16x16x32_bf16 v[140:143], v[244:247], v[76:79], v[140:143]\n\t"
    "v_mfma_f32_16x16x32_bf16 v[144:147], v[224:227], v[68:71], v[144:147]\n\t"
    "v_mfma_f32_16x16x32_bf16 v[144:147], v[236:239], v[72:75], v[144:147]\n\t"
    "v_mfma_f32_16x16x32_bf16 v[144:147], v[248:251], v[76:79], v[144:147]\n\t"
    "v_mfma_f32_16x16x32_bf16 v[148:151], v[228:231], v[68:71], v[148:151]\n\t"
    "v_mfma_f32_16x16x32_bf16 v[148:151], v[240:243], v[72:75], v[148:151]\n\t"
    "v_mfma_f32_16x16x32_bf16 v[148:151], v[252:255], v[76:79], v[148:151]\n\t"
    "v_mfma_f32_16x16x32_bf16 v[176:179], a[180:183], v[68:71], v[176:179]\n\t"
    "ds_read_b128 v[60:63], v34 offset:3136\n\t"
    "v_mfma_f32_16x16x32_bf16 v[176:179], a[192:195], v[72:75], v[176:179]\n\t"
    "s_nop 0\n\t"
    "v_cvt_pk_bf16_f32 v38, v140, v141\n\t"
    "v_mfma_f32_16x16x32_bf16 v[176:179], a[204:207], v[76:79], v[176:179]\n\t"
    "v_cvt_pk_bf16_f32 v39, v142, v143\n\t"
    "v_mfma_f32_16x16x32_bf16 v[180:183], a[184:187], v[68:71], v[180:183]\n\t"
    "v_cvt_pk_bf16_f32 v40, v144, v145\n\t"
    "v_mfma_f32_16x16x32_bf16 v[180:183], a[196:199], v[72:75], v[180:183]\n\t"
    "v_cvt_pk_bf16_f32 v41, v146, v147\n\t"
    "v_mfma_f32_16x16x32_bf16 v[180:183], a[208:211], v[76:79], v[180:183]\n\t"
    "ds_write_b128 v32, v[38:41] offset:6144\n\t"
    "v_mfma_f32_16x16x32_bf16 v[184:187], a[188:191], v[68:71], v[184:187]\n\t"
    "s_nop 1\n\t"
    "v_cvt_pk_bf16_f32 v42, v148, v149\n\t"
    "v_mfma_f32_16x16x32_bf16 v[184:187], a[200:203], v[72:75], v[184:187]\n\t"
    "v_cvt_pk_bf16_f32 v43, v150, v151\n\t"
    "v_mfma_f32_16x16x32_bf16 v[184:187], a[212:215], v[76:79], v[184:187]\n\t"
    "ds_write_b64 v33, v[42:43] offset:6144\n\t"
    "v_mfma_f32_16x16x32_bf16 v[104:107], a[72:75], v[68:71], v[104:107]\n\t"
    "ds_read_b128 v[64:67], v34 offset:3200\n\t"
    "v_mfma_f32_16x16x32_bf16 v[104:107], a[84:87], v[72:75], v[104:107]\n\t"
    "v_mfma_f32_16x16x32_bf16 v[104:107], a[96:99], v[76:79], v[104:107]\n\t"
    "v_mfma_f32_16x16x32_bf16 v[108:111], a[76:79], v[68:71], v[108:111]\n\t"
    "v_mfma_f32_16x16x32_bf16 v[108:111], a[88:91], v[72:75], v[108:111]\n\t"
    "v_mfma_f32_16x16x32_bf16 v[108:111], a[100:103], v[76:79], v[108:111]\n\t"
    "v_mfma_f32_16x16x32_bf16 v[112:115], a[80:83], v[68:71], v[112:115]\n\t"
    "v_mfma_f32_16x16x32_bf16 v[112:115], a[92:95], v[72:75], v[112:115]\n\t"
    "v_mfma_f32_16x16x32_bf16 v[112:115], a[104:107], v[76:79], v[112:115]\n\t"
    "; ---- unit 54: row 4 j 0 b 0 ds [2, 1, 0]\n\t"
    "s_waitcnt lgkmcnt(5)\n\t"
    "v_mfma_f32_16x16x32_bf16 v[152:155], a[216:219], v[44:47], v[152:155]\n\t"
    "ds_read_b128 v[68:71], v34 offset:6144\n\t"
    "v_mfma_f32_16x16x32_bf16 v[152:155], a[228:231], v[48:51], v[152:155]\n\t"
    "v_mfma_f32_16x16x32_bf16 v[152:155], a[240:243], v[52:55], v[152:155]\n\t"
    "v_mfma_f32_16x16x32_bf16 v[156:159], a[220:223], v[44:47], v[156:159]\n\t"
    "v_mfma_f32_16x16x32_bf16 v[156:159], a[232:235], v[48:51], v[156:159]\n\t"
    "v_mfma_f32_16x16x32_bf16 v[156:159], a[244:247], v[52:55], v[156:159]\n\t"
    "v_mfma_f32_16x16x32_bf16 v[160:163], a[224:227], v[44:47], v[160:163]\n\t"
    "v_mfma_f32_16x16x32_bf16 v[160:163], a[236:239], v[48:51], v[160:163]\n\t"
    "v_mfma_f32_16x16x32_bf16 v[160:163], a[248:251], v[52:55], v[160:163]\n\t"
    "v_mfma_f32_16x16x32_bf16 v[80:83], a[108:111], v[44:47], v[80:83]\n\t"
    "ds_read_b128 v[72:75], v34 offset:6208\n\t"
    "v_mfma_f32_16x16x32_bf16 v[80:83], a[120:123], v[48:51], v[80:83]\n\t"
    "v_mfma_f32_16x16x32_bf16 v[80:83], a[132:135], v[52:55], v[80:83]\n\t"
    "v_mfma_f32_16x16x32_bf16 v[84:87], a[112:115], v[44:47], v[84:87]\n\t"
    "v_mfma_f32_16x16x32_bf16 v[84:87], a[124:127], v[48:51], v[84:87]\n\t"
    "v_mfma_f32_16x16x32_bf16 v[84:87], a[136:139], v[52:55], v[84:87]\n\t"
    "v_mfma_f32_16x16x32_bf16 v[88:91], a[116:119], v[44:47], v[88:91]\n\t"
    "v_mfma_f32_16x16x32_bf16 v[88:91], a[128:131], v[48:51], v[88:91]\n\t"
    "v_mfma_f32_16x16x32_bf16 v[88:91], a[140:143], v[52:55], v[88:91]\n\t"
    "v_mfma_f32_16x16x32_bf16 v[116:119], a[0:3], v[44:47], 0\n\t"
    "ds_read_b128 v[76:79], v34 offset:6272\n\t"
    "v_mfma_f32_16x16x32_bf16 v[116:119], a[12:15], v[48:51], v[116:119]\n\t"
    "v_mfma_f32_16x16x32_bf16 v[116:119], a[24:27], v[52:55], v[116:119]\n\t"
    "v_mfma_f32_16x16x32_bf16 v[120:123], a[4:7], v[44:47], 0\n\t"
    "v_mfma_f32_16x16x32_bf16 v[120:123], a[16:19], v[48:51], v[120:123]\n\t"
    "v_mfma_f32_16x16x32_bf16 v[120:123], a[28:31], v[52:55], v[120:123]\n\t"
    "v_mfma_f32_16x16x32_bf16 v[124:127], a[8:11], v[44:47], 0\n\t"
    "v_mfma_f32_16x16x32_bf16 v[124:127], a[20:23], v[48:51], v[124:127]\n\t"
    "v_mfma_f32_16x16x32_bf16 v[124:127], a[32:35], v[52:55], v[124:127]\n\t"
    "; ---- unit 55: row 4 j 0 b 1 ds [2, 1, 0]\n\t"
    "s_waitcnt lgkmcnt(3)\n\t"
    "v_mfma_f32_16x16x32_bf16 v[164:167], a[216:219], v[56:59], v[164:167]\n\t"
    "v_add_u32 v35, s49, v12\n\t"
    "v_mfma_f32_16x16x32_bf16 v[164:167], a[228:231], v[60:63], v[164:167]\n\t"
    "v_mfma_f32_16x16x32_bf16 v[164:167], a[240:243], v[64:67], v[164:167]\n\t"
    "v_mfma_f32_16x16x32_bf16 v[168:171], a[220:223], v[56:59], v[168:171]\n\t"
    "v_mfma_f32_16x16x32_bf16 v[168:171], a[232:235], v[60:63], v[168:171]\n\t"
    "v_mfma_f32_16x16x32_bf16 v[168:171], a[244:247], v[64:67], v[168:171]\n\t"
    "v_mfma_f32_16x16x32_bf16 v[172:175], a[224:227], v[56:59], v[172:175]\n\t"
    "ds_read_b128 v[44:47], v35 offset:0\n\t"
    "v_mfma_f32_16x16x32_bf16 v[172:175], a[236:239], v[60:63], v[172:175]\n\t"
    "v_mfma_f32_16x16x32_bf16 v[172:175], a[248:251], v[64:67], v[172:175]\n\t"
    "v_mfma_f32_16x16x32_bf16 v[92:95], a[108:111], v[56:59], v[92:95]\n\t"
    "v_mfma_f32_16x16x32_bf16 v[92:95], a[120:123], v[60:63], v[92:95]\n\t"
    "v_mfma_f32_16x16x32_bf16 v[92:95], a[132:135], v[64:67], v[92:95]\n\t"
    "v_mfma_f32_16x16x32_bf16 v[96:99], a[112:115], v[56:59], v[96:99]\n\t"
    "v_mfma_f32_16x16x32_bf16 v[96:99], a[124:127], v[60:63], v[96:99]\n\t"
    "ds_read_b128 v[48:51], v35 offset:64\n\t"
    "v_mfma_f32_16x16x32_bf16 v[96:99], a[136:139], v[64:67], v[96:99]\n\t"
    "v_mfma_f32_16x16x32_bf16 v[100:103], a[116:119], v[56:59], v[100:103]\n\t"
    "v_mfma_f32_16x16x32_bf16 v[100:103], a[128:131], v[60:63], v[100:103]\n\t"
    "v_mfma_f32_16x16x32_bf16 v[100:103], a[140:143], v[64:67], v[100:103]\n\t"
    "v_mfma_f32_16x16x32_bf16 v[128:131], a[0:3], v[56:59], 0\n\t"
    "v_mfma_f32_16x16x32_bf16 v[128:131], a[12:15], v[60:63], v[128:131]\n\t"
    "v_mfma_f32_16x16x32_bf16 v[128:131], a[24:27], v[64:67], v[128:131]\n\t"
    "ds_read_b128 v[52:55], v35 offset:128\n\t"
    "v_mfma_f32_16x16x32_bf16 v[132:135], a[4:7], v[56:59], 0\n\t"
    "v_mfma_f32_16x16x32_bf16 v[132:135], a[16:19], v[60:63], v[132:135]\n\t"
    "v_mfma_f32_16x16x32_bf16 v[132:135], a[28:31], v[64:67], v[132:135]\n\t"
    "v_mfma_f32_16x16x32_bf16 v[136:139], a[8:11], v[56:59], 0\n\t"
    "v_mfma_f32_16x16x32_bf16 v[136:139], a[20:23], v[60:63], v[136:139]\n\t"
    "v_mfma_f32_16x16x32_bf16 v[136:139], a[32:35], v[64:67], v[136:139]\n\t"
    "; ---- unit 56: row 4 j 0 b 2 ds [2, 1, 0]\n\t"
    "s_waitcnt vmcnt(0)\n\t"
    "s_barrier\n\t"
    "s_waitcnt lgkmcnt(3)\n\t"
    "v_mfma_f32_16x16x32_bf16 v[176:179], a[216:219], v[68:71], v[176:179]\n\t"
    "ds_read_b128 v[56:59], v35 offset:3072\n\t"
    "v_mfma_f32_16x16x32_bf16 v[176:179], a[228:231], v[72:75], v[176:179]\n\t"
    "v_add_u32 v23, 40960, v18\n\t"
    "v_mfma_f32_16x16x32_bf16 v[176:179], a[240:243], v[76:79], v[176:179]\n\t"
    "s_add_u32 s42, %[oso], 147456\n\t"
    "v_mfma_f32_16x16x32_bf16 v[180:183], a[220:223], v[68:71], v[180:183]\n\t"
    "ds_read_b128 v[24:27], v23 offset:0\n\t"
    "v_mfma_f32_16x16x32_bf16 v[180:183], a[232:235], v[72:75], v[180:183]\n\t"
    "ds_read_b128 v[28:31], v23 offset:1024\n\t"
    "v_mfma_f32_16x16x32_bf16 v[180:183], a[244:247], v[76:79], v[180:183]\n\t"
    "v_mfma_f32_16x16x32_bf16 v[184:187], a[224:227], v[68:71], v[184:187]\n\t"
    "v_mfma_f32_16x16x32_bf16 v[184:187], a[236:239], v[72:75], v[184:187]\n\t"
    "v_mfma_f32_16x16x32_bf16 v[184:187], a[248:251], v[76:79], v[184:187]\n\t"
    "v_mfma_f32_16x16x32_bf16 v[104:107], a[108:111], v[68:71], v[104:107]\n\t"
    "ds_read_b128 v[60:63], v35 offset:3136\n\t"
    "v_mfma_f32_16x16x32_bf16 v[104:107], a[120:123], v[72:75], v[104:107]\n\t"
    "s_waitcnt lgkmcnt(2)\n\t"
    "buffer_store_dwordx4 v[24:27], v17, %[srdo], s42 offen offset:0 sc1\n\t"
    "v_mfma_f32_16x16x32_bf16 v[104:107], a[132:135], v[76:79], v[104:107]\n\t"
    "ds_read_b128 v[24:27], v23 offset:2048\n\t"
    "v_mfma_f32_16x16x32_bf16 v[108:111], a[112:115], v[68:71], v[108:111]\n\t"
    "s_waitcnt lgkmcnt(2)\n\t"
    "buffer_store_dwordx4 v[28:31], v17, %[srdo], s42 offen offset:1024 sc1\n\t"
    "v_mfma_f32_16x16x32_bf16 v[108:111], a[124:127], v[72:75], v[108:111]\n\t"
    "ds_read_b128 v[28:31], v23 offset:3072\n\t"
    "v_mfma_f32_16x16x32_bf16 v[108:111], a[136:139], v[76:79], v[108:111]\n\t"
    "v_mfma_f32_16x16x32_bf16 v[112:115], a[116:119], v[68:71], v[112:115]\n\t"
    "v_mfma_f32_16x16x32_bf16 v[112:115], a[128:131], v[72:75], v[112:115]\n\t"
    "v_mfma_f32_16x16x32_bf16 v[112:115], a[140:143], v[76:79], v[112:115]\n\t"
    "v_mfma_f32_16x16x32_bf16 v[140:143], a[0:3], v[68:71], 0\n\t"
    "ds_read_b128 v[64:67], v35 offset:3200\n\t"
    "v_mfma_f32_16x16x32_bf16 v[140:143], a[12:15], v[72:75], v[140:143]\n\t"
    "s_waitcnt lgkmcnt(2)\n\t"
    "buffer_store_dwordx4 v[24:27], v17, %[srdo], s42 offen offset:2048 sc1\n\t"
    "v_mfma_f32_16x16x32_bf16 v[140:143], a[24:27], v[76:79], v[140:143]\n\t"
    "ds_read_b128 v[24:27], v23 offset:4096\n\t"
    "v_mfma_f32_16x16x32_bf16 v[144:147], a[4:7], v[68:71], 0\n\t"
    "s_waitcnt lgkmcnt(2)\n\t"
    "buffer_store_dwordx4 v[28:31], v17, %[srdo], s42 offen offset:3072 sc1\n\t"
    "v_mfma_f32_16x16x32_bf16 v[144:147], a[16:19], v[72:75], v[144:147]\n\t"
    "v_mfma_f32_16x16x32_bf16 v[144:147], a[28:31], v[76:79], v[144:147]\n\t"
    "v_mfma_f32_16x16x32_bf16 v[148:151], a[8:11], v[68:71], 0\n\t"
    "v_mfma_f32_16x16x32_bf16 v[148:151], a[20:23], v[72:75], v[148:151]\n\t"
    "v_mfma_f32_16x16x32_bf16 v[148:151], a[32:35], v[76:79], v[148:151]\n\t"
    "; ---- unit 57: row 4 j 1 b 0 ds [2, 1, 0]\n\t"
    "v_mfma_f32_16x16x32_bf16 v[152:155], a[252:255], v[44:47], v[152:155]\n\t"
    "ds_read_b128 v[68:71], v35 offset:6144\n\t"
    "v_mfma_f32_16x16x32_bf16 v[152:155], v[196:199], v[48:51], v[152:155]\n\t"
    "s_waitcnt lgkmcnt(1)\n\t"
    "buffer_store_dwordx4 v[24:27], v16, %[srdo], s42 offen sc1\n\t"
    "v_mfma_f32_16x16x32_bf16 v[152:155], v[208:211], v[52:55], v[152:155]\n\t"
    "s_add_u32 s44, %[xsol], 0\n\t"
    "v_mfma_f32_16x16x32_bf16 v[156:159], v[188:191], v[44:47], v[156:159]\n\t"
    "s_add_u32 m0, %[m0x], 102400\n\t"
    "v_mfma_f32_16x16x32_bf16 v[156:159], v[200:203], v[48:51], v[156:159]\n\t"
    "buffer_load_dwordx4 %[vx0], %[srdx], s44 offen offset:0 lds\n\t"
    "v_mfma_f32_16x16x32_bf16 v[156:159], v[212:215], v[52:55], v[156:159]\n\t"
    "buffer_load_dwordx4 %[vx1], %[srdx], s44 offen offset:1024 lds\n\t"
    "v_mfma_f32_16x16x32_bf16 v[160:163], v[192:195], v[44:47], v[160:163]\n\t"
    "buffer_load_dwordx4 %[vx2], %[srdx], s44 offen offset:2048 lds\n\t"
    "v_mfma_f32_16x16x32_bf16 v[160:163], v[204:207], v[48:51], v[160:163]\n\t"
    "buffer_load_dwordx4 %[vx3], %[srdx], s44 offen offset:3072 lds\n\t"
    "v_mfma_f32_16x16x32_bf16 v[160:163], v[216:219], v[52:55], v[160:163]\n\t"
    "s_add_u32 m0, %[m0x], 106496\n\t"
    "v_mfma_f32_16x16x32_bf16 v[80:83], a[144:147], v[44:47], v[80:83]\n\t"
    "ds_read_b128 v[72:75], v35 offset:6208\n\t"
    "v_mfma_f32_16x16x32_bf16 v[80:83], a[156:159], v[48:51], v[80:83]\n\t"
    "buffer_load_dwordx4 %[vx4], %[srdx], s44 offen lds\n\t"
    "v_mfma_f32_16x16x32_bf16 v[80:83], a[168:171], v[52:55], v[80:83]\n\t"
    "v_mfma_f32_16x16x32_bf16 v[84:87], a[148:151], v[44:47], v[84:87]\n\t"
    "v_mfma_f32_16x16x32_bf16 v[84:87], a[160:163], v[48:51], v[84:87]\n\t"
    "v_mfma_f32_16x16x32_bf16 v[84:87], a[172:175], v[52:55], v[84:87]\n\t"
    "v_mfma_f32_16x16x32_bf16 v[88:91], a[152:155], v[44:47], v[88:91]\n\t"
    "v_mfma_f32_16x16x32_bf16 v[88:91], a[164:167], v[48:51], v[88:91]\n\t"
    "v_mfma_f32_16x16x32_bf16 v[88:91], a[176:179], v[52:55], v[88:91]\n\t"
    "v_mfma_f32_16x16x32_bf16 v[116:119], a[36:39], v[44:47], v[116:119]\n\t"
    "ds_read_b128 v[76:79], v35 offset:6272\n\t"
    "v_mfma_f32_16x16x32_bf16 v[116:119], a[48:51], v[48:51], v[116:119]\n\t"
    "v_mfma_f32_16x16x32_bf16 v[116:119], a[60:63], v[52:55], v[116:119]\n\t"
    "v_mfma_f32_16x16x32_bf16 v[120:123], a[40:43], v[44:47], v[120:123]\n\t"
    "v_mfma_f32_16x16x32_bf16 v[120:123], a[52:55], v[48:51], v[120:123]\n\t"
    "v_mfma_f32_16x16x32_bf16 v[120:123], a[64:67], v[52:55], v[120:123]\n\t"
    "v_mfma_f32_16x16x32_bf16 v[124:127], a[44:47], v[44:47], v[124:127]\n\t"
    "v_mfma_f32_16x16x32_bf16 v[124:127], a[56:59], v[48:51], v[124:127]\n\t"
    "v_mfma_f32_16x16x32_bf16 v[124:127], a[68:71], v[52:55], v[124:127]\n\t"
    "; ---- unit 58: row 4 j 1 b 1 ds [2, 1, 0]\n\t"
    "v_mfma_f32_16x16x32_bf16 v[164:167], a[252:255], v[56:59], v[164:167]\n\t"
    "v_add_u32 v36, s49, v13\n\t"
    "v_mfma_f32_16x16x32_bf16 v[164:167], v[196:199], v[60:63], v[164:167]\n\t"
    "v_mfma_f32_16x16x32_bf16 v[164:167], v[208:211], v[64:67], v[164:167]\n\t"
    "v_mfma_f32_16x16x32_bf16 v[168:171], v[188:191], v[56:59], v[168:171]\n\t"
    "v_mfma_f32_16x16x32_bf16 v[168:171], v[200:203], v[60:63], v[168:171]\n\t"
    "v_mfma_f32_16x16x32_bf16 v[168:171], v[212:215], v[64:67], v[168:171]\n\t"
    "v_mfma_f32_16x16x32_bf16 v[172:175], v[192:195], v[56:59], v[172:175]\n\t"
    "ds_read_b128 v[44:47], v36 offset:0\n\t"
    "v_mfma_f32_16x16x32_bf16 v[172:175], v[204:207], v[60:63], v[172:175]\n\t"
    "v_mfma_f32_16x16x32_bf16 v[172:175], v[216:219], v[64:67], v[172:175]\n\t"
    "v_mfma_f32_16x16x32_bf16 v[92:95], a[144:147], v[56:59], v[92:95]\n\t"
    "v_mfma_f32_16x16x32_bf16 v[92:95], a[156:159], v[60:63], v[92:95]\n\t"
    "v_mfma_f32_16x16x32_bf16 v[92:95], a[168:171], v[64:67], v[92:95]\n\t"
    "v_mfma_f32_16x16x32_bf16 v[96:99], a[148:151], v[56:59], v[96:99]\n\t"
    "v_mfma_f32_16x16x32_bf16 v[96:99], a[160:163], v[60:63], v[96:99]\n\t"
    "ds_read_b128 v[48:51], v36 offset:64\n\t"
    "v_mfma_f32_16x16x32_bf16 v[96:99], a[172:175], v[64:67], v[96:99]\n\t"
    "v_mfma_f32_16x16x32_bf16 v[100:103], a[152:155], v[56:59], v[100:103]\n\t"
    "v_mfma_f32_16x16x32_bf16 v[100:103], a[164:167], v[60:63], v[100:103]\n\t"
    "v_mfma_f32_16x16x32_bf16 v[100:103], a[176:179], v[64:67], v[100:103]\n\t"
    "v_mfma_f32_16x16x32_bf16 v[128:131], a[36:39], v[56:59], v[128:131]\n\t"
    "v_mfma_f32_16x16x32_bf16 v[128:131], a[48:51], v[60:63], v[128:131]\n\t"
    "v_mfma_f32_16x16x32_bf16 v[128:131], a[60:63], v[64:67], v[128:131]\n\t"
    "ds_read_b128 v[52:55], v36 offset:128\n\t"
    "v_mfma_f32_16x16x32_bf16 v[132:135], a[40:43], v[56:59], v[132:135]\n\t"
    "v_mfma_f32_16x16x32_bf16 v[132:135], a[52:55], v[60:63], v[132:135]\n\t"
    "v_mfma_f32_16x16x32_bf16 v[132:135], a[64:67], v[64:67], v[132:135]\n\t"
    "v_mfma_f32_16x16x32_bf16 v[136:139], a[44:47], v[56:59], v[136:139]\n\t"
    "v_mfma_f32_16x16x32_bf16 v[136:139], a[56:59], v[60:63], v[136:139]\n\t"
    "v_mfma_f32_16x16x32_bf16 v[136:139], a[68:71], v[64:67], v[136:139]\n\t"
    "; ---- unit 59: row 4 j 1 b 2 ds [2, 1, 0]\n\t"
    "s_waitcnt lgkmcnt(3)\n\t"
    "v_mfma_f32_16x16x32_bf16 v[176:179], a[252:255], v[68:71], v[176:179]\n\t"
    "ds_read_b128 v[56:59], v36 offset:3072\n\t"
    "v_mfma_f32_16x16x32_bf16 v[176:179], v[196:199], v[72:75], v[176:179]\n\t"
    "v_mfma_f32_16x16x32_bf16 v[176:179], v[208:211], v[76:79], v[176:179]\n\t"
    "v_mfma_f32_16x16x32_bf16 v[180:183], v[188:191], v[68:71], v[180:183]\n\t"
    "v_mfma_f32_16x16x32_bf16 v[180:183], v[200:203], v[72:75], v[180:183]\n\t"
    "v_mfma_f32_16x16x32_bf16 v[180:183], v[212:215], v[76:79], v[180:183]\n\t"
    "v_mfma_f32_16x16x32_bf16 v[184:187], v[192:195], v[68:71], v[184:187]\n\t"
    "v_mfma_f32_16x16x32_bf16 v[184:187], v[204:207], v[72:75], v[184:187]\n\t"
    "v_mfma_f32_16x16x32_bf16 v[184:187], v[216:219], v[76:79], v[184:187]\n\t"
    "v_mfma_f32_16x16x32_bf16 v[104:107], a[144:147], v[68:71], v[104:107]\n\t"
    "ds_read_b128 v[60:63], v36 offset:3136\n\t"
    "v_mfma_f32_16x16x32_bf16 v[104:107], a[156:159], v[72:75], v[104:107]\n\t"
    "v_mfma_f32_16x16x32_bf16 v[104:107], a[168:171], v[76:79], v[104:107]\n\t"
    "v_mfma_f32_16x16x32_bf16 v[108:111], a[148:151], v[68:71], v[108:111]\n\t"
    "v_mfma_f32_16x16x32_bf16 v[108:111], a[160:163], v[72:75], v[108:111]\n\t"
    "v_mfma_f32_16x16x32_bf16 v[108:111], a[172:175], v[76:79], v[108:111]\n\t"
    "v_mfma_f32_16x16x32_bf16 v[112:115], a[152:155], v[68:71], v[112:115]\n\t"
    "v_mfma_f32_16x16x32_bf16 v[112:115], a[164:167], v[72:75], v[112:115]\n\t"
    "v_mfma_f32_16x16x32_bf16 v[112:115], a[176:179], v[76:79], v[112:115]\n\t"
    "v_mfma_f32_16x16x32_bf16 v[140:143], a[36:39], v[68:71], v[140:143]\n\t"
    "ds_read_b128 v[64:67], v36 offset:3200\n\t"
    "v_mfma_f32_16x16x32_bf16 v[140:143], a[48:51], v[72:75], v[140:143]\n\t"
    "v_mfma_f32_16x16x32_bf16 v[140:143], a[60:63], v[76:79], v[140:143]\n\t"
    "v_mfma_f32_16x16x32_bf16 v[144:147], a[40:43], v[68:71], v[144:147]\n\t"
    "v_mfma_f32_16x16x32_bf16 v[144:147], a[52:55], v[72:75], v[144:147]\n\t"
    "v_mfma_f32_16x16x32_bf16 v[144:147], a[64:67], v[76:79], v[144:147]\n\t"
    "v_mfma_f32_16x16x32_bf16 v[148:151], a[44:47], v[68:71], v[148:151]\n\t"
    "v_mfma_f32_16x16x32_bf16 v[148:151], a[56:59], v[72:75], v[148:151]\n\t"
    "v_mfma_f32_16x16x32_bf16 v[148:151], a[68:71], v[76:79], v[148:151]\n\t"
    "; ---- unit 60: row 4 j 2 b 0 ds [2, 1, 0]\n\t"
    "s_waitcnt lgkmcnt(3)\n\t"
    "v_mfma_f32_16x16x32_bf16 v[152:155], v[220:223], v[44:47], v[152:155]\n\t"
    "ds_read_b128 v[68:71], v36 offset:6144\n\t"
    "v_mfma_f32_16x16x32_bf16 v[152:155], v[232:235], v[48:51], v[152:155]\n\t"
    "v_mfma_f32_16x16x32_bf16 v[152:155], v[244:247], v[52:55], v[152:155]\n\t"
    "v_mfma_f32_16x16x32_bf16 v[156:159], v[224:227], v[44:47], v[156:159]\n\t"
    "v_mfma_f32_16x16x32_bf16 v[156:159], v[236:239], v[48:51], v[156:159]\n\t"
    "v_mfma_f32_16x16x32_bf16 v[156:159], v[248:251], v[52:55], v[156:159]\n\t"
    "v_mfma_f32_16x16x32_bf16 v[160:163], v[228:231], v[44:47], v[160:163]\n\t"
    "v_mfma_f32_16x16x32_bf16 v[160:163], v[240:243], v[48:51], v[160:163]\n\t"
    "v_mfma_f32_16x16x32_bf16 v[160:163], v[252:255], v[52:55], v[160:163]\n\t"
    "v_mfma_f32_16x16x32_bf16 v[80:83], a[180:183], v[44:47], v[80:83]\n\t"
    "ds_read_b128 v[72:75], v36 offset:6208\n\t"
    "v_mfma_f32_16x16x32_bf16 v[80:83], a[192:195], v[48:51], v[80:83]\n\t"
    "v_add_u32 v32, 122880, v20\n\t"
    "v_mfma_f32_16x16x32_bf16 v[80:83], a[204:207], v[52:55], v[80:83]\n\t"
    "v_add_u32 v33, 122880, v19\n\t"
    "v_mfma_f32_16x16x32_bf16 v[84:87], a[184:187], v[44:47], v[84:87]\n\t"
    "v_cvt_pk_bf16_f32 v38, v152, v153\n\t"
    "v_mfma_f32_16x16x32_bf16 v[84:87], a[196:199], v[48:51], v[84:87]\n\t"
    "v_cvt_pk_bf16_f32 v39, v154, v155\n\t"
    "v_mfma_f32_16x16x32_bf16 v[84:87], a[208:211], v[52:55], v[84:87]\n\t"
    "v_cvt_pk_bf16_f32 v40, v156, v157\n\t"
    "v_mfma_f32_16x16x32_bf16 v[88:91], a[188:191], v[44:47], v[88:91]\n\t"
    "v_cvt_pk_bf16_f32 v41, v158, v159\n\t"
    "v_mfma_f32_16x16x32_bf16 v[88:91], a[200:203], v[48:51], v[88:91]\n\t"
    "ds_write_b128 v32, v[38:41] offset:0\n\t"
    "v_mfma_f32_16x16x32_bf16 v[88:91], a[212:215], v[52:55], v[88:91]\n\t"
    "s_nop 1\n\t"
    "v_cvt_pk_bf16_f32 v42, v160, v161\n\t"
    "v_mfma_f32_16x16x32_bf16 v[116:119], a[72:75], v[44:47], v[116:119]\n\t"
    "ds_read_b128 v[76:79], v36 offset:6272\n\t"
    "v_mfma_f32_16x16x32_bf16 v[116:119], a[84:87], v[48:51], v[116:119]\n\t"
    "v_cvt_pk_bf16_f32 v43, v162, v163\n\t"
    "v_mfma_f32_16x16x32_bf16 v[116:119], a[96:99], v[52:55], v[116:119]\n\t"
    "ds_write_b64 v33, v[42:43] offset:0\n\t"
    "v_mfma_f32_16x16x32_bf16 v[120:123], a[76:79], v[44:47], v[120:123]\n\t"
    "v_mfma_f32_16x16x32_bf16 v[120:123], a[88:91], v[48:51], v[120:123]\n\t"
    "v_mfma_f32_16x16x32_bf16 v[120:123], a[100:103], v[52:55], v[120:123]\n\t"
    "v_mfma_f32_16x16x32_bf16 v[124:127], a[80:83], v[44:47], v[124:127]\n\t"
    "v_mfma_f32_16x16x32_bf16 v[124:127], a[92:95], v[48:51], v[124:127]\n\t"
    "v_mfma_f32_16x16x32_bf16 v[124:127], a[104:107], v[52:55], v[124:127]\n\t"
    "; ---- unit 61: row 4 j 2 b 1 ds [2, 1, 0]\n\t"
    "s_waitcnt lgkmcnt(5)\n\t"
    "v_mfma_f32_16x16x32_bf16 v[164:167], v[220:223], v[56:59], v[164:167]\n\t"
    "v_add_u32 v37, s50, v11\n\t"
    "v_mfma_f32_16x16x32_bf16 v[164:167], v[232:235], v[60:63], v[164:167]\n\t"
    "v_mfma_f32_16x16x32_bf16 v[164:167], v[244:247], v[64:67], v[164:167]\n\t"
    "v_mfma_f32_16x16x32_bf16 v[168:171], v[224:227], v[56:59], v[168:171]\n\t"
    "v_mfma_f32_16x16x32_bf16 v[168:171], v[236:239], v[60:63], v[168:171]\n\t"
    "v_mfma_f32_16x16x32_bf16 v[168:171], v[248:251], v[64:67], v[168:171]\n\t"
    "v_mfma_f32_16x16x32_bf16 v[172:175], v[228:231], v[56:59], v[172:175]\n\t"
    "ds_read_b128 v[44:47], v37 offset:0\n\t"
    "v_mfma_f32_16x16x32_bf16 v[172:175], v[240:243], v[60:63], v[172:175]\n\t"
    "v_mfma_f32_16x16x32_bf16 v[172:175], v[252:255], v[64:67], v[172:175]\n\t"
    "v_mfma_f32_16x16x32_bf16 v[92:95], a[180:183], v[56:59], v[92:95]\n\t"
    "s_nop 1\n\t"
    "v_cvt_pk_bf16_f32 v38, v164, v165\n\t"
    "v_mfma_f32_16x16x32_bf16 v[92:95], a[192:195], v[60:63], v[92:95]\n\t"
    "v_cvt_pk_bf16_f32 v39, v166, v167\n\t"
    "v_mfma_f32_16x16x32_bf16 v[92:95], a[204:207], v[64:67], v[92:95]\n\t"
    "v_cvt_pk_bf16_f32 v40, v168, v169\n\t"
    "v_mfma_f32_16x16x32_bf16 v[96:99], a[184:187], v[56:59], v[96:99]\n\t"
    "v_cvt_pk_bf16_f32 v41, v170, v171\n\t"
    "v_mfma_f32_16x16x32_bf16 v[96:99], a[196:199], v[60:63], v[96:99]\n\t"
    "ds_read_b128 v[48:51], v37 offset:64\n\t"
    "v_mfma_f32_16x16x32_bf16 v[96:99], a[208:211], v[64:67], v[96:99]\n\t"
    "ds_write_b128 v32, v[38:41] offset:3072\n\t"
    "v_mfma_f32_16x16x32_bf16 v[100:103], a[188:191], v[56:59], v[100:103]\n\t"
    "s_nop 1\n\t"
    "v_cvt_pk_bf16_f32 v42, v172, v173\n\t"
    "v_mfma_f32_16x16x32_bf16 v[100:103], a[200:203], v[60:63], v[100:103]\n\t"
    "v_cvt_pk_bf16_f32 v43, v174, v175\n\t"
    "v_mfma_f32_16x16x32_bf16 v[100:103], a[212:215], v[64:67], v[100:103]\n\t"
    "ds_write_b64 v33, v[42:43] offset:3072\n\t"
    "v_mfma_f32_16x16x32_bf16 v[128:131], a[72:75], v[56:59], v[128:131]\n\t"
    "v_mfma_f32_16x16x32_bf16 v[128:131], a[84:87], v[60:63], v[128:131]\n\t"
    "v_mfma_f32_16x16x32_bf16 v[128:131], a[96:99], v[64:67], v[128:131]\n\t"
    "ds_read_b128 v[52:55], v37 offset:128\n\t"
    "v_mfma_f32_16x16x32_bf16 v[132:135], a[76:79], v[56:59], v[132:135]\n\t"
    "v_mfma_f32_16x16x32_bf16 v[132:135], a[88:91], v[60:63], v[132:135]\n\t"
    "v_mfma_f32_16x16x32_bf16 v[132:135], a[100:103], v[64:67], v[132:135]\n\t"
    "v_mfma_f32_16x16x32_bf16 v[136:139], a[80:83], v[56:59], v[136:139]\n\t"
    "v_mfma_f32_16x16x32_bf16 v[136:139], a[92:95], v[60:63], v[136:139]\n\t"
    "v_mfma_f32_16x16x32_bf16 v[136:139], a[104:107], v[64:67], v[136:139]\n\t"
    "; ---- unit 62: row 4 j 2 b 2 ds [2, 1, 0]\n\t"
    "s_waitcnt lgkmcnt(6)\n\t"
    "v_mfma_f32_16x16x32_bf16 v[176:179], v[220:223], v[68:71], v[176:179]\n\t"
    "ds_read_b128 v[56:59], v37 offset:3072\n\t"
    "v_mfma_f32_16x16x32_bf16 v[176:179], v[232:235], v[72:75], v[176:179]\n\t"
    "v_mfma_f32_16x16x32_bf16 v[176:179], v[244:247], v[76:79], v[176:179]\n\t"
    "v_mfma_f32_16x16x32_bf16 v[180:183], v[224:227], v[68:71], v[180:183]\n\t"
    "v_mfma_f32_16x16x32_bf16 v[180:183], v[236:239], v[72:75], v[180:183]\n\t"
    "v_mfma_f32_16x16x32_bf16 v[180:183], v[248:251], v[76:79], v[180:183]\n\t"
    "v_mfma_f32_16x16x32_bf16 v[184:187], v[228:231], v[68:71], v[184:187]\n\t"
    "v_mfma_f32_16x16x32_bf16 v[184:187], v[240:243], v[72:75], v[184:187]\n\t"
    "v_mfma_f32_16x16x32_bf16 v[184:187], v[252:255], v[76:79], v[184:187]\n\t"
    "v_mfma_f32_16x16x32_bf16 v[104:107], a[180:183], v[68:71], v[104:107]\n\t"
    "ds_read_b128 v[60:63], v37 offset:3136\n\t"
    "v_mfma_f32_16x16x32_bf16 v[104:107], a[192:195], v[72:75], v[104:107]\n\t"
    "s_nop 0\n\t"
    "v_cvt_pk_bf16_f32 v38, v176, v177\n\t"
    "v_mfma_f32_16x16x32_bf16 v[104:107], a[204:207], v[76:79], v[104:107]\n\t"
    "v_cvt_pk_bf16_f32 v39, v178, v179\n\t"
    "v_mfma_f32_16x16x32_bf16 v[108:111], a[184:187], v[68:71], v[108:111]\n\t"
    "v_cvt_pk_bf16_f32 v40, v180, v181\n\t"
    "v_mfma_f32_16x16x32_bf16 v[108:111], a[196:199], v[72:75], v[108:111]\n\t"
    "v_cvt_pk_bf16_f32 v41, v182, v183\n\t"
    "v_mfma_f32_16x16x32_bf16 v[108:111], a[208:211], v[76:79], v[108:111]\n\t"
    "ds_write_b128 v32, v[38:41] offset:6144\n\t"
    "v_mfma_f32_16x16x32_bf16 v[112:115], a[188:191], v[68:71], v[112:115]\n\t"
    "s_nop 1\n\t"
    "v_cvt_pk_bf16_f32 v42, v184, v185\n\t"
    "v_mfma_f32_16x16x32_bf16 v[112:115], a[200:203], v[72:75], v[112:115]\n\t"
    "v_cvt_pk_bf16_f32 v43, v186, v187\n\t"
    "v_mfma_f32_16x16x32_bf16 v[112:115], a[212:215], v[76:79], v[112:115]\n\t"
    "ds_write_b64 v33, v[42:43] offset:6144\n\t"
    "v_mfma_f32_16x16x32_bf16 v[140:143], a[72:75], v[68:71], v[140:143]\n\t"
    "ds_read_b128 v[64:67], v37 offset:3200\n\t"
    "v_mfma_f32_16x16x32_bf16 v[140:143], a[84:87], v[72:75], v[140:143]\n\t"
    "v_mfma_f32_16x16x32_bf16 v[140:143], a[96:99], v[76:79], v[140:143]\n\t"
    "v_mfma_f32_16x16x32_bf16 v[144:147], a[76:79], v[68:71], v[144:147]\n\t"
    "v_mfma_f32_16x16x32_bf16 v[144:147], a[88:91], v[72:75], v[144:147]\n\t"
    "v_mfma_f32_16x16x32_bf16 v[144:147], a[100:103], v[76:79], v[144:147]\n\t"
    "v_mfma_f32_16x16x32_bf16 v[148:151], a[80:83], v[68:71], v[148:151]\n\t"
    "v_mfma_f32_16x16x32_bf16 v[148:151], a[92:95], v[72:75], v[148:151]\n\t"
    "v_mfma_f32_16x16x32_bf16 v[148:151], a[104:107], v[76:79], v[148:151]\n\t"
    "; ---- unit 63: row 5 j 0 b 0 ds [2, 1, 0]\n\t"
    "s_waitcnt lgkmcnt(5)\n\t"
    "v_mfma_f32_16x16x32_bf16 v[80:83], a[216:219], v[44:47], v[80:83]\n\t"
    "ds_read_b128 v[68:71], v37 offset:6144\n\t"
    "v_mfma_f32_16x16x32_bf16 v[80:83], a[228:231], v[48:51], v[80:83]\n\t"
    "v_mfma_f32_16x16x32_bf16 v[80:83], a[240:243], v[52:55], v[80:83]\n\t"
    "v_mfma_f32_16x16x32_bf16 v[84:87], a[220:223], v[44:47], v[84:87]\n\t"
    "v_mfma_f32_16x16x32_bf16 v[84:87], a[232:235], v[48:51], v[84:87]\n\t"
    "v_mfma_f32_16x16x32_bf16 v[84:87], a[244:247], v[52:55], v[84:87]\n\t"
    "v_mfma_f32_16x16x32_bf16 v[88:91], a[224:227], v[44:47], v[88:91]\n\t"
    "v_mfma_f32_16x16x32_bf16 v[88:91], a[236:239], v[48:51], v[88:91]\n\t"
    "v_mfma_f32_16x16x32_bf16 v[88:91], a[248:251], v[52:55], v[88:91]\n\t"
    "v_mfma_f32_16x16x32_bf16 v[116:119], a[108:111], v[44:47], v[116:119]\n\t"
    "ds_read_b128 v[72:75], v37 offset:6208\n\t"
    "v_mfma_f32_16x16x32_bf16 v[116:119], a[120:123], v[48:51], v[116:119]\n\t"
    "v_mfma_f32_16x16x32_bf16 v[116:119], a[132:135], v[52:55], v[116:119]\n\t"
    "v_mfma_f32_16x16x32_bf16 v[120:123], a[112:115], v[44:47], v[120:123]\n\t"
    "v_mfma_f32_16x16x32_bf16 v[120:123], a[124:127], v[48:51], v[120:123]\n\t"
    "v_mfma_f32_16x16x32_bf16 v[120:123], a[136:139], v[52:55], v[120:123]\n\t"
    "v_mfma_f32_16x16x32_bf16 v[124:127], a[116:119], v[44:47], v[124:127]\n\t"
    "v_mfma_f32_16x16x32_bf16 v[124:127], a[128:131], v[48:51], v[124:127]\n\t"
    "v_mfma_f32_16x16x32_bf16 v[124:127], a[140:143], v[52:55], v[124:127]\n\t"
    "v_mfma_f32_16x16x32_bf16 v[152:155], a[0:3], v[44:47], 0\n\t"
    "ds_read_b128 v[76:79], v37 offset:6272\n\t"
    "v_mfma_f32_16x16x32_bf16 v[152:155], a[12:15], v[48:51], v[152:155]\n\t"
    "v_mfma_f32_16x16x32_bf16 v[152:155], a[24:27], v[52:55], v[152:155]\n\t"
    "v_mfma_f32_16x16x32_bf16 v[156:159], a[4:7], v[44:47], 0\n\t"
    "v_mfma_f32_16x16x32_bf16 v[156:159], a[16:19], v[48:51], v[156:159]\n\t"
    "v_mfma_f32_16x16x32_bf16 v[156:159], a[28:31], v[52:55], v[156:159]\n\t"
    "v_mfma_f32_16x16x32_bf16 v[160:163], a[8:11], v[44:47], 0\n\t"
    "v_mfma_f32_16x16x32_bf16 v[160:163], a[20:23], v[48:51], v[160:163]\n\t"
    "v_mfma_f32_16x16x32_bf16 v[160:163], a[32:35], v[52:55], v[160:163]\n\t"
    "; ---- unit 64: row 5 j 0 b 1 ds [2, 1, 0]\n\t"
    "s_waitcnt lgkmcnt(3)\n\t"
    "v_mfma_f32_16x16x32_bf16 v[92:95], a[216:219], v[56:59], v[92:95]\n\t"
    "v_add_u32 v34, s50, v12\n\t"
    "v_mfma_f32_16x16x32_bf16 v[92:95], a[228:231], v[60:63], v[92:95]\n\t"
    "v_mfma_f32_16x16x32_bf16 v[92:95], a[240:243], v[64:67], v[92:95]\n\t"
    "v_mfma_f32_16x16x32_bf16 v[96:99], a[220:223], v[56:59], v[96:99]\n\t"
    "v_mfma_f32_16x16x32_bf16 v[96:99], a[232:235], v[60:63], v[96:99]\n\t"
    "v_mfma_f32_16x16x32_bf16 v[96:99], a[244:247], v[64:67], v[96:99]\n\t"
    "v_mfma_f32_16x16x32_bf16 v[100:103], a[224:227], v[56:59], v[100:103]\n\t"
    "ds_read_b128 v[44:47], v34 offset:0\n\t"
    "v_mfma_f32_16x16x32_bf16 v[100:103], a[236:239], v[60:63], v[100:103]\n\t"
    "v_mfma_f32_16x16x32_bf16 v[100:103], a[248:251], v[64:67], v[100:103]\n\t"
    "v_mfma_f32_16x16x32_bf16 v[128:131], a[108:111], v[56:59], v[128:131]\n\t"
    "v_mfma_f32_16x16x32_bf16 v[128:131], a[120:123], v[60:63], v[128:131]\n\t"
    "v_mfma_f32_16x16x32_bf16 v[128:131], a[132:135], v[64:67], v[128:131]\n\t"
    "v_mfma_f32_16x16x32_bf16 v[132:135], a[112:115], v[56:59], v[132:135]\n\t"
    "v_mfma_f32_16x16x32_bf16 v[132:135], a[124:127], v[60:63], v[132:135]\n\t"
    "ds_read_b128 v[48:51], v34 offset:64\n\t"
    "v_mfma_f32_16x16x32_bf16 v[132:135], a[136:139], v[64:67], v[132:135]\n\t"
    "v_mfma_f32_16x16x32_bf16 v[136:139], a[116:119], v[56:59], v[136:139]\n\t"
    "v_mfma_f32_16x16x32_bf16 v[136:139], a[128:131], v[60:63], v[136:139]\n\t"
    "v_mfma_f32_16x16x32_bf16 v[136:139], a[140:143], v[64:67], v[136:139]\n\t"
    "v_mfma_f32_16x16x32_bf16 v[164:167], a[0:3], v[56:59], 0\n\t"
    "v_mfma_f32_16x16x32_bf16 v[164:167], a[12:15], v[60:63], v[164:167]\n\t"
    "v_mfma_f32_16x16x32_bf16 v[164:167], a[24:27], v[64:67], v[164:167]\n\t"
    "ds_read_b128 v[52:55], v34 offset:128\n\t"
    "v_mfma_f32_16x16x32_bf16 v[168:171], a[4:7], v[56:59], 0\n\t"
    "v_mfma_f32_16x16x32_bf16 v[168:171], a[16:19], v[60:63], v[168:171]\n\t"
    "v_mfma_f32_16x16x32_bf16 v[168:171], a[28:31], v[64:67], v[168:171]\n\t"
    "v_mfma_f32_16x16x32_bf16 v[172:175], a[8:11], v[56:59], 0\n\t"
    "v_mfma_f32_16x16x32_bf16 v[172:175], a[20:23], v[60:63], v[172:175]\n\t"
    "v_mfma_f32_16x16x32_bf16 v[172:175], a[32:35], v[64:67], v[172:175]\n\t"
    "; ---- unit 65: row 5 j 0 b 2 ds [2, 1, 0]\n\t"
    "s_waitcnt vmcnt(0)\n\t"
    "s_barrier\n\t"
    "s_waitcnt lgkmcnt(3)\n\t"
    "v_mfma_f32_16x16x32_bf16 v[104:107], a[216:219], v[68:71], v[104:107]\n\t"
    "ds_read_b128 v[56:59], v34 offset:3072\n\t"
    "v_mfma_f32_16x16x32_bf16 v[104:107], a[228:231], v[72:75], v[104:107]\n\t"
    "v_add_u32 v23, 122880, v18\n\t"
    "v_mfma_f32_16x16x32_bf16 v[104:107], a[240:243], v[76:79], v[104:107]\n\t"
    "s_add_u32 s42, %[oso], 294912\n\t"
    "v_mfma_f32_16x16x32_bf16 v[108:111], a[220:223], v[68:71], v[108:111]\n\t"
    "ds_read_b128 v[24:27], v23 offset:0\n\t"
    "v_mfma_f32_16x16x32_bf16 v[108:111], a[232:235], v[72:75], v[108:111]\n\t"
    "ds_read_b128 v[28:31], v23 offset:1024\n\t"
    "v_mfma_f32_16x16x32_bf16 v[108:111], a[244:247], v[76:79], v[108:111]\n\t"
    "v_mfma_f32_16x16x32_bf16 v[112:115], a[224:227], v[68:71], v[112:115]\n\t"
    "v_mfma_f32_16x16x32_bf16 v[112:115], a[236:239], v[72:75], v[112:115]\n\t"
    "v_mfma_f32_16x16x32_bf16 v[112:115], a[248:251], v[76:79], v[112:115]\n\t"
    "v_mfma_f32_16x16x32_bf16 v[140:143], a[108:111], v[68:71], v[140:143]\n\t"
    "ds_read_b128 v[60:63], v34 offset:3136\n\t"
    "v_mfma_f32_16x16x32_bf16 v[140:143], a[120:123], v[72:75], v[140:143]\n\t"
    "s_waitcnt lgkmcnt(2)\n\t"
    "buffer_store_dwordx4 v[24:27], v17, %[srdo], s42 offen offset:0 sc1\n\t"
    "v_mfma_f32_16x16x32_bf16 v[140:143], a[132:135], v[76:79], v[140:143]\n\t"
    "ds_read_b128 v[24:27], v23 offset:2048\n\t"
    "v_mfma_f32_16x16x32_bf16 v[144:147], a[112:115], v[68:71], v[144:147]\n\t"
    "s_waitcnt lgkmcnt(2)\n\t"
    "buffer_store_dwordx4 v[28:31], v17, %[srdo], s42 offen offset:1024 sc1\n\t"
    "v_mfma_f32_16x16x32_bf16 v[144:147], a[124:127], v[72:75], v[144:147]\n\t"
    "ds_read_b128 v[28:31], v23 offset:3072\n\t"
    "v_mfma_f32_16x16x32_bf16 v[144:147], a[136:139], v[76:79], v[144:147]\n\t"
    "v_mfma_f32_16x16x32_bf16 v[148:151], a[116:119], v[68:71], v[148:151]\n\t"
    "v_mfma_f32_16x16x32_bf16 v[148:151], a[128:131], v[72:75], v[148:151]\n\t"
    "v_mfma_f32_16x16x32_bf16 v[148:151], a[140:143], v[76:79], v[148:151]\n\t"
    "v_mfma_f32_16x16x32_bf16 v[176:179], a[0:3], v[68:71], 0\n\t"
    "ds_read_b128 v[64:67], v34 offset:3200\n\t"
    "v_mfma_f32_16x16x32_bf16 v[176:179], a[12:15], v[72:75], v[176:179]\n\t"
    "s_waitcnt lgkmcnt(2)\n\t"
    "buffer_store_dwordx4 v[24:27], v17, %[srdo], s42 offen offset:2048 sc1\n\t"
    "v_mfma_f32_16x16x32_bf16 v[176:179], a[24:27], v[76:79], v[176:179]\n\t"
    "ds_read_b128 v[24:27], v23 offset:4096\n\t"
    "v_mfma_f32_16x16x32_bf16 v[180:183], a[4:7], v[68:71], 0\n\t"
    "s_waitcnt lgkmcnt(2)\n\t"
    "buffer_store_dwordx4 v[28:31], v17, %[srdo], s42 offen offset:3072 sc1\n\t"
    "v_mfma_f32_16x16x32_bf16 v[180:183], a[16:19], v[72:75], v[180:183]\n\t"
    "v_mfma_f32_16x16x32_bf16 v[180:183], a[28:31], v[76:79], v[180:183]\n\t"
    "v_mfma_f32_16x16x32_bf16 v[184:187], a[8:11], v[68:71], 0\n\t"
    "v_mfma_f32_16x16x32_bf16 v[184:187], a[20:23], v[72:75], v[184:187]\n\t"
    "v_mfma_f32_16x16x32_bf16 v[184:187], a[32:35], v[76:79], v[184:187]\n\t"
    "; ---- unit 66: row 5 j 1 b 0 ds [2, 1, 0]\n\t"
    "v_mfma_f32_16x16x32_bf16 v[80:83], a[252:255], v[44:47], v[80:83]\n\t"
    "ds_read_b128 v[68:71], v34 offset:6144\n\t"
    "v_mfma_f32_16x16x32_bf16 v[80:83], v[196:199], v[48:51], v[80:83]\n\t"
    "s_waitcnt lgkmcnt(1)\n\t"
    "buffer_store_dwordx4 v[24:27], v16, %[srdo], s42 offen sc1\n\t"
    "v_mfma_f32_16x16x32_bf16 v[80:83], v[208:211], v[52:55], v[80:83]\n\t"
    "v_mfma_f32_16x16x32_bf16 v[84:87], v[188:191], v[44:47], v[84:87]\n\t"
    "v_mfma_f32_16x16x32_bf16 v[84:87], v[200:203], v[48:51], v[84:87]\n\t"
    "v_mfma_f32_16x16x32_bf16 v[84:87], v[212:215], v[52:55], v[84:87]\n\t"
    "v_mfma_f32_16x16x32_bf16 v[88:91], v[192:195], v[44:47], v[88:91]\n\t"
    "v_mfma_f32_16x16x32_bf16 v[88:91], v[204:207], v[48:51], v[88:91]\n\t"
    "v_mfma_f32_16x16x32_bf16 v[88:91], v[216:219], v[52:55], v[88:91]\n\t"
    "v_mfma_f32_16x16x32_bf16 v[116:119], a[144:147], v[44:47], v[116:119]\n\t"
    "ds_read_b128 v[72:75], v34 offset:6208\n\t"
    "v_mfma_f32_16x16x32_bf16 v[116:119], a[156:159], v[48:51], v[116:119]\n\t"
    "v_mfma_f32_16x16x32_bf16 v[116:119], a[168:171], v[52:55], v[116:119]\n\t"
    "v_mfma_f32_16x16x32_bf16 v[120:123], a[148:151], v[44:47], v[120:123]\n\t"
    "v_mfma_f32_16x16x32_bf16 v[120:123], a[160:163], v[48:51], v[120:123]\n\t"
    "v_mfma_f32_16x16x32_bf16 v[120:123], a[172:175], v[52:55], v[120:123]\n\t"
    "v_mfma_f32_16x16x32_bf16 v[124:127], a[152:155], v[44:47], v[124:127]\n\t"
    "v_mfma_f32_16x16x32_bf16 v[124:127], a[164:167], v[48:51], v[124:127]\n\t"
    "v_mfma_f32_16x16x32_bf16 v[124:127], a[176:179], v[52:55], v[124:127]\n\t"
    "v_mfma_f32_16x16x32_bf16 v[152:155], a[36:39], v[44:47], v[152:155]\n\t"
    "ds_read_b128 v[76:79], v34 offset:6272\n\t"
    "v_mfma_f32_16x16x32_bf16 v[152:155], a[48:51], v[48:51], v[152:155]\n\t"
    "v_mfma_f32_16x16x32_bf16 v[152:155], a[60:63], v[52:55], v[152:155]\n\t"
    "v_mfma_f32_16x16x32_bf16 v[156:159], a[40:43], v[44:47], v[156:159]\n\t"
    "v_mfma_f32_16x16x32_bf16 v[156:159], a[52:55], v[48:51], v[156:159]\n\t"
    "v_mfma_f32_16x16x32_bf16 v[156:159], a[64:67], v[52:55], v[156:159]\n\t"
    "v_mfma_f32_16x16x32_bf16 v[160:163], a[44:47], v[44:47], v[160:163]\n\t"
    "v_mfma_f32_16x16x32_bf16 v[160:163], a[56:59], v[48:51], v[160:163]\n\t"
    "v_mfma_f32_16x16x32_bf16 v[160:163], a[68:71], v[52:55], v[160:163]\n\t"
    "; ---- unit 67: row 5 j 1 b 1 ds [2, 1, 0]\n\t"
    "v_mfma_f32_16x16x32_bf16 v[92:95], a[252:255], v[56:59], v[92:95]\n\t"
    "v_add_u32 v35, s50, v13\n\t"
    "v_mfma_f32_16x16x32_bf16 v[92:95], v[196:199], v[60:63], v[92:95]\n\t"
    "v_mfma_f32_16x16x32_bf16 v[92:95], v[208:211], v[64:67], v[92:95]\n\t"
    "v_mfma_f32_16x16x32_bf16 v[96:99], v[188:191], v[56:59], v[96:99]\n\t"
    "v_mfma_f32_16x16x32_bf16 v[96:99], v[200:203], v[60:63], v[96:99]\n\t"
    "v_mfma_f32_16x16x32_bf16 v[96:99], v[212:215], v[64:67], v[96:99]\n\t"
    "v_mfma_f32_16x16x32_bf16 v[100:103], v[192:195], v[56:59], v[100:103]\n\t"
    "ds_read_b128 v[44:47], v35 offset:0\n\t"
    "v_mfma_f32_16x16x32_bf16 v[100:103], v[204:207], v[60:63], v[100:103]\n\t"
    "v_mfma_f32_16x16x32_bf16 v[100:103], v[216:219], v[64:67], v[100:103]\n\t"
    "v_mfma_f32_16x16x32_bf16 v[128:131], a[144:147], v[56:59], v[128:131]\n\t"
    "v_mfma_f32_16x16x32_bf16 v[128:131], a[156:159], v[60:63], v[128:131]\n\t"
    "v_mfma_f32_16x16x32_bf16 v[128:131], a[168:171], v[64:67], v[128:131]\n\t"
    "v_mfma_f32_16x16x32_bf16 v[132:135], a[148:151], v[56:59], v[132:135]\n\t"
    "v_mfma_f32_16x16x32_bf16 v[132:135], a[160:163], v[60:63], v[132:135]\n\t"
    "ds_read_b128 v[48:51], v35 offset:64\n\t"
    "v_mfma_f32_16x16x32_bf16 v[132:135], a[172:175], v[64:67], v[132:135]\n\t"
    "v_mfma_f32_16x16x32_bf16 v[136:139], a[152:155], v[56:59], v[136:139]\n\t"
    "v_mfma_f32_16x16x32_bf16 v[136:139], a[164:167], v[60:63], v[136:139]\n\t"
    "v_mfma_f32_16x16x32_bf16 v[136:139], a[176:179], v[64:67], v[136:139]\n\t"
    "v_mfma_f32_16x16x32_bf16 v[164:167], a[36:39], v[56:59], v[164:167]\n\t"
    "v_mfma_f32_16x16x32_bf16 v[164:167], a[48:51], v[60:63], v[164:167]\n\t"
    "v_mfma_f32_16x16x32_bf16 v[164:167], a[60:63], v[64:67], v[164:167]\n\t"
    "ds_read_b128 v[52:55], v35 offset:128\n\t"
    "v_mfma_f32_16x16x32_bf16 v[168:171], a[40:43], v[56:59], v[168:171]\n\t"
    "v_mfma_f32_16x16x32_bf16 v[168:171], a[52:55], v[60:63], v[168:171]\n\t"
    "v_mfma_f32_16x16x32_bf16 v[168:171], a[64:67], v[64:67], v[168:171]\n\t"
    "v_mfma_f32_16x16x32_bf16 v[172:175], a[44:47], v[56:59], v[172:175]\n\t"
    "v_mfma_f32_16x16x32_bf16 v[172:175], a[56:59], v[60:63], v[172:175]\n\t"
    "v_mfma_f32_16x16x32_bf16 v[172:175], a[68:71], v[64:67], v[172:175]\n\t"
    "; ---- unit 68: row 5 j 1 b 2 ds [2, 1, 0]\n\t"
    "s_waitcnt lgkmcnt(3)\n\t"
    "v_mfma_f32_16x16x32_bf16 v[104:107], a[252:255], v[68:71], v[104:107]\n\t"
    "ds_read_b128 v[56:59], v35 offset:3072\n\t"
    "v_mfma_f32_16x16x32_bf16 v[104:107], v[196:199], v[72:75], v[104:107]\n\t"
    "v_mfma_f32_16x16x32_bf16 v[104:107], v[208:211], v[76:79], v[104:107]\n\t"
    "v_mfma_f32_16x16x32_bf16 v[108:111], v[188:191], v[68:71], v[108:111]\n\t"
    "v_mfma_f32_16x16x32_bf16 v[108:111], v[200:203], v[72:75], v[108:111]\n\t"
    "v_mfma_f32_16x16x32_bf16 v[108:111], v[212:215], v[76:79], v[108:111]\n\t"
    "v_mfma_f32_16x16x32_bf16 v[112:115], v[192:195], v[68:71], v[112:115]\n\t"
    "v_mfma_f32_16x16x32_bf16 v[112:115], v[204:207], v[72:75], v[112:115]\n\t"
    "v_mfma_f32_16x16x32_bf16 v[112:115], v[216:219], v[76:79], v[112:115]\n\t"
    "v_mfma_f32_16x16x32_bf16 v[140:143], a[144:147], v[68:71], v[140:143]\n\t"
    "ds_read_b128 v[60:63], v35 offset:3136\n\t"
    "v_mfma_f32_16x16x32_bf16 v[140:143], a[156:159], v[72:75], v[140:143]\n\t"
    "v_mfma_f32_16x16x32_bf16 v[140:143], a[168:171], v[76:79], v[140:143]\n\t"
    "v_mfma_f32_16x16x32_bf16 v[144:147], a[148:151], v[68:71], v[144:147]\n\t"
    "v_mfma_f32_16x16x32_bf16 v[144:147], a[160:163], v[72:75], v[144:147]\n\t"
    "v_mfma_f32_16x16x32_bf16 v[144:147], a[172:175], v[76:79], v[144:147]\n\t"
    "v_mfma_f32_16x16x32_bf16 v[148:151], a[152:155], v[68:71], v[148:151]\n\t"
    "v_mfma_f32_16x16x32_bf16 v[148:151], a[164:167], v[72:75], v[148:151]\n\t"
    "v_mfma_f32_16x16x32_bf16 v[148:151], a[176:179], v[76:79], v[148:151]\n\t"
    "v_mfma_f32_16x16x32_bf16 v[176:179], a[36:39], v[68:71], v[176:179]\n\t"
    "ds_read_b128 v[64:67], v35 offset:3200\n\t"
    "v_mfma_f32_16x16x32_bf16 v[176:179], a[48:51], v[72:75], v[176:179]\n\t"
    "v_mfma_f32_16x16x32_bf16 v[176:179], a[60:63], v[76:79], v[176:179]\n\t"
    "v_mfma_f32_16x16x32_bf16 v[180:183], a[40:43], v[68:71], v[180:183]\n\t"
    "v_mfma_f32_16x16x32_bf16 v[180:183], a[52:55], v[72:75], v[180:183]\n\t"
    "v_mfma_f32_16x16x32_bf16 v[180:183], a[64:67], v[76:79], v[180:183]\n\t"
    "v_mfma_f32_16x16x32_bf16 v[184:187], a[44:47], v[68:71], v[184:187]\n\t"
    "v_mfma_f32_16x16x32_bf16 v[184:187], a[56:59], v[72:75], v[184:187]\n\t"
    "v_mfma_f32_16x16x32_bf16 v[184:187], a[68:71], v[76:79], v[184:187]\n\t"
    "; ---- unit 69: row 5 j 2 b 0 ds [2, 1, 0]\n\t"
    "s_waitcnt lgkmcnt(3)\n\t"
    "v_mfma_f32_16x16x32_bf16 v[80:83], v[220:223], v[44:47], v[80:83]\n\t"
    "ds_read_b128 v[68:71], v35 offset:6144\n\t"
    "v_mfma_f32_16x16x32_bf16 v[80:83], v[232:235], v[48:51], v[80:83]\n\t"
    "v_mfma_f32_16x16x32_bf16 v[80:83], v[244:247], v[52:55], v[80:83]\n\t"
    "v_mfma_f32_16x16x32_bf16 v[84:87], v[224:227], v[44:47], v[84:87]\n\t"
    "v_mfma_f32_16x16x32_bf16 v[84:87], v[236:239], v[48:51], v[84:87]\n\t"
    "v_mfma_f32_16x16x32_bf16 v[84:87], v[248:251], v[52:55], v[84:87]\n\t"
    "v_mfma_f32_16x16x32_bf16 v[88:91], v[228:231], v[44:47], v[88:91]\n\t"
    "v_mfma_f32_16x16x32_bf16 v[88:91], v[240:243], v[48:51], v[88:91]\n\t"
    "v_mfma_f32_16x16x32_bf16 v[88:91], v[252:255], v[52:55], v[88:91]\n\t"
    "v_mfma_f32_16x16x32_bf16 v[116:119], a[180:183], v[44:47], v[116:119]\n\t"
    "ds_read_b128 v[72:75], v35 offset:6208\n\t"
    "v_mfma_f32_16x16x32_bf16 v[116:119], a[192:195], v[48:51], v[116:119]\n\t"
    "v_add_u32 v32, 40960, v20\n\t"
    "v_mfma_f32_16x16x32_bf16 v[116:119], a[204:207], v[52:55], v[116:119]\n\t"
    "v_add_u32 v33, 40960, v19\n\t"
    "v_mfma_f32_16x16x32_bf16 v[120:123], a[184:187], v[44:47], v[120:123]\n\t"
    "v_cvt_pk_bf16_f32 v38, v80, v81\n\t"
    "v_mfma_f32_16x16x32_bf16 v[120:123], a[196:199], v[48:51], v[120:123]\n\t"
    "v_cvt_pk_bf16_f32 v39, v82, v83\n\t"
    "v_mfma_f32_16x16x32_bf16 v[120:123], a[208:211], v[52:55], v[120:123]\n\t"
    "v_cvt_pk_bf16_f32 v40, v84, v85\n\t"
    "v_mfma_f32_16x16x32_bf16 v[124:127], a[188:191], v[44:47], v[124:127]\n\t"
    "v_cvt_pk_bf16_f32 v41, v86, v87\n\t"
    "v_mfma_f32_16x16x32_bf16 v[124:127], a[200:203], v[48:51], v[124:127]\n\t"
    "ds_write_b128 v32, v[38:41] offset:0\n\t"
    "v_mfma_f32_16x16x32_bf16 v[124:127], a[212:215], v[52:55], v[124:127]\n\t"
    "s_nop 1\n\t"
    "v_cvt_pk_bf16_f32 v42, v88, v89\n\t"
    "v_mfma_f32_16x16x32_bf16 v[152:155], a[72:75], v[44:47], v[152:155]\n\t"
    "ds_read_b128 v[76:79], v35 offset:6272\n\t"
    "v_mfma_f32_16x16x32_bf16 v[152:155], a[84:87], v[48:51], v[152:155]\n\t"
    "v_cvt_pk_bf16_f32 v43, v90, v91\n\t"
    "v_mfma_f32_16x16x32_bf16 v[152:155], a[96:99], v[52:55], v[152:155]\n\t"
    "ds_write_b64 v33, v[42:43] offset:0\n\t"
    "v_mfma_f32_16x16x32_bf16 v[156:159], a[76:79], v[44:47], v[156:159]\n\t"
    "v_mfma_f32_16x16x32_bf16 v[156:159], a[88:91], v[48:51], v[156:159]\n\t"
    "v_mfma_f32_16x16x32_bf16 v[156:159], a[100:103], v[52:55], v[156:159]\n\t"
    "v_mfma_f32_16x16x32_bf16 v[160:163], a[80:83], v[44:47], v[160:163]\n\t"
    "v_mfma_f32_16x16x32_bf16 v[160:163], a[92:95], v[48:51], v[160:163]\n\t"
    "v_mfma_f32_16x16x32_bf16 v[160:163], a[104:107], v[52:55], v[160:163]\n\t"
    "; ---- unit 70: row 5 j 2 b 1 ds [2, 1, 0]\n\t"
    "s_waitcnt lgkmcnt(5)\n\t"
    "v_mfma_f32_16x16x32_bf16 v[92:95], v[220:223], v[56:59], v[92:95]\n\t"
    "v_add_u32 v36, s51, v11\n\t"
    "v_mfma_f32_16x16x32_bf16 v[92:95], v[232:235], v[60:63], v[92:95]\n\t"
    "v_mfma_f32_16x16x32_bf16 v[92:95], v[244:247], v[64:67], v[92:95]\n\t"
    "v_mfma_f32_16x16x32_bf16 v[96:99], v[224:227], v[56:59], v[96:99]\n\t"
    "v_mfma_f32_16x16x32_bf16 v[96:99], v[236:239], v[60:63], v[96:99]\n\t"
    "v_mfma_f32_16x16x32_bf16 v[96:99], v[248:251], v[64:67], v[96:99]\n\t"
    "v_mfma_f32_16x16x32_bf16 v[100:103], v[228:231], v[56:59], v[100:103]\n\t"
    "ds_read_b128 v[44:47], v36 offset:0\n\t"
    "v_mfma_f32_16x16x32_bf16 v[100:103], v[240:243], v[60:63], v[100:103]\n\t"
    "v_mfma_f32_16x16x32_bf16 v[100:103], v[252:255], v[64:67], v[100:103]\n\t"
    "v_mfma_f32_16x16x32_bf16 v[128:131], a[180:183], v[56:59], v[128:131]\n\t"
    "s_nop 1\n\t"
    "v_cvt_pk_bf16_f32 v38, v92, v93\n\t"
    "v_mfma_f32_16x16x32_bf16 v[128:131], a[192:195], v[60:63], v[128:131]\n\t"
    "v_cvt_pk_bf16_f32 v39, v94, v95\n\t"
    "v_mfma_f32_16x16x32_bf16 v[128:131], a[204:207], v[64:67], v[128:131]\n\t"
    "v_cvt_pk_bf16_f32 v40, v96, v97\n\t"
    "v_mfma_f32_16x16x32_bf16 v[132:135], a[184:187], v[56:59], v[132:135]\n\t"
    "v_cvt_pk_bf16_f32 v41, v98, v99\n\t"
    "v_mfma_f32_16x16x32_bf16 v[132:135], a[196:199], v[60:63], v[132:135]\n\t"
    "ds_read_b128 v[48:51], v36 offset:64\n\t"
    "v_mfma_f32_16x16x32_bf16 v[132:135], a[208:211], v[64:67], v[132:135]\n\t"
    "ds_write_b128 v32, v[38:41] offset:3072\n\t"
    "v_mfma_f32_16x16x32_bf16 v[136:139], a[188:191], v[56:59], v[136:139]\n\t"
    "s_nop 1\n\t"
    "v_cvt_pk_bf16_f32 v42, v100, v101\n\t"
    "v_mfma_f32_16x16x32_bf16 v[136:139], a[200:203], v[60:63], v[136:139]\n\t"
    "v_cvt_pk_bf16_f32 v43, v102, v103\n\t"
    "v_mfma_f32_16x16x32_bf16 v[136:139], a[212:215], v[64:67], v[136:139]\n\t"
    "ds_write_b64 v33, v[42:43] offset:3072\n\t"
    "v_mfma_f32_16x16x32_bf16 v[164:167], a[72:75], v[56:59], v[164:167]\n\t"
    "v_mfma_f32_16x16x32_bf16 v[164:167], a[84:87], v[60:63], v[164:167]\n\t"
    "v_mfma_f32_16x16x32_bf16 v[164:167], a[96:99], v[64:67], v[164:167]\n\t"
    "ds_read_b128 v[52:55], v36 offset:128\n\t"
    "v_mfma_f32_16x16x32_bf16 v[168:171], a[76:79], v[56:59], v[168:171]\n\t"
    "v_mfma_f32_16x16x32_bf16 v[168:171], a[88:91], v[60:63], v[168:171]\n\t"
    "v_mfma_f32_16x16x32_bf16 v[168:171], a[100:103], v[64:67], v[168:171]\n\t"
    "v_mfma_f32_16x16x32_bf16 v[172:175], a[80:83], v[56:59], v[172:175]\n\t"
    "v_mfma_f32_16x16x32_bf16 v[172:175], a[92:95], v[60:63], v[172:175]\n\t"
    "v_mfma_f32_16x16x32_bf16 v[172:175], a[104:107], v[64:67], v[172:175]\n\t"
    "; ---- unit 71: row 5 j 2 b 2 ds [2, 1, 0]\n\t"
    "s_waitcnt lgkmcnt(6)\n\t"
    "v_mfma_f32_16x16x32_bf16 v[104:107], v[220:223], v[68:71], v[104:107]\n\t"
    "ds_read_b128 v[56:59], v36 offset:3072\n\t"
    "v_mfma_f32_16x16x32_bf16 v[104:107], v[232:235], v[72:75], v[104:107]\n\t"
    "v_mfma_f32_16x16x32_bf16 v[104:107], v[244:247], v[76:79], v[104:107]\n\t"
    "v_mfma_f32_16x16x32_bf16 v[108:111], v[224:227], v[68:71], v[108:111]\n\t"
    "v_mfma_f32_16x16x32_bf16 v[108:111], v[236:239], v[72:75], v[108:111]\n\t"
    "v_mfma_f32_16x16x32_bf16 v[108:111], v[248:251], v[76:79], v[108:111]\n\t"
    "v_mfma_f32_16x16x32_bf16 v[112:115], v[228:231], v[68:71], v[112:115]\n\t"
    "v_mfma_f32_16x16x32_bf16 v[112:115], v[240:243], v[72:75], v[112:115]\n\t"
    "v_mfma_f32_16x16x32_bf16 v[112:115], v[252:255], v[76:79], v[112:115]\n\t"
    "v_mfma_f32_16x16x32_bf16 v[140:143], a[180:183], v[68:71], v[140:143]\n\t"
    "ds_read_b128 v[60:63], v36 offset:3136\n\t"
    "v_mfma_f32_16x16x32_bf16 v[140:143], a[192:195], v[72:75], v[140:143]\n\t"
    "s_nop 0\n\t"
    "v_cvt_pk_bf16_f32 v38, v104, v105\n\t"
    "v_mfma_f32_16x16x32_bf16 v[140:143], a[204:207], v[76:79], v[140:143]\n\t"
    "v_cvt_pk_bf16_f32 v39, v106, v107\n\t"
    "v_mfma_f32_16x16x32_bf16 v[144:147], a[184:187], v[68:71], v[144:147]\n\t"
    "v_cvt_pk_bf16_f32 v40, v108, v109\n\t"
    "v_mfma_f32_16x16x32_bf16 v[144:147], a[196:199], v[72:75], v[144:147]\n\t"
    "v_cvt_pk_bf16_f32 v41, v110, v111\n\t"
    "v_mfma_f32_16x16x32_bf16 v[144:147], a[208:211], v[76:79], v[144:147]\n\t"
    "ds_write_b128 v32, v[38:41] offset:6144\n\t"
    "v_mfma_f32_16x16x32_bf16 v[148:151], a[188:191], v[68:71], v[148:151]\n\t"
    "s_nop 1\n\t"
    "v_cvt_pk_bf16_f32 v42, v112, v113\n\t"
    "v_mfma_f32_16x16x32_bf16 v[148:151], a[200:203], v[72:75], v[148:151]\n\t"
    "v_cvt_pk_bf16_f32 v43, v114, v115\n\t"
    "v_mfma_f32_16x16x32_bf16 v[148:151], a[212:215], v[76:79], v[148:151]\n\t"
    "ds_write_b64 v33, v[42:43] offset:6144\n\t"
    "v_mfma_f32_16x16x32_bf16 v[176:179], a[72:75], v[68:71], v[176:179]\n\t"
    "ds_read_b128 v[64:67], v36 offset:3200\n\t"
    "v_mfma_f32_16x16x32_bf16 v[176:179], a[84:87], v[72:75], v[176:179]\n\t"
    "v_mfma_f32_16x16x32_bf16 v[176:179], a[96:99], v[76:79], v[176:179]\n\t"
    "v_mfma_f32_16x16x32_bf16 v[180:183], a[76:79], v[68:71], v[180:183]\n\t"
    "v_mfma_f32_16x16x32_bf16 v[180:183], a[88:91], v[72:75], v[180:183]\n\t"
    "v_mfma_f32_16x16x32_bf16 v[180:183], a[100:103], v[76:79], v[180:183]\n\t"
    "v_mfma_f32_16x16x32_bf16 v[184:187], a[80:83], v[68:71], v[184:187]\n\t"
    "v_mfma_f32_16x16x32_bf16 v[184:187], a[92:95], v[72:75], v[184:187]\n\t"
    "v_mfma_f32_16x16x32_bf16 v[184:187], a[104:107], v[76:79], v[184:187]\n\t"
    "; ---- unit 72: row 6 j 0 b 0 ds [2]\n\t"
    "s_waitcnt lgkmcnt(5)\n\t"
    "v_mfma_f32_16x16x32_bf16 v[116:119], a[216:219], v[44:47], v[116:119]\n\t"
    "ds_read_b128 v[68:71], v36 offset:6144\n\t"
    "v_mfma_f32_16x16x32_bf16 v[116:119], a[228:231], v[48:51], v[116:119]\n\t"
    "v_mfma_f32_16x16x32_bf16 v[116:119], a[240:243], v[52:55], v[116:119]\n\t"
    "v_mfma_f32_16x16x32_bf16 v[120:123], a[220:223], v[44:47], v[120:123]\n\t"
    "ds_read_b128 v[72:75], v36 offset:6208\n\t"
    "v_mfma_f32_16x16x32_bf16 v[120:123], a[232:235], v[48:51], v[120:123]\n\t"
    "v_mfma_f32_16x16x32_bf16 v[120:123], a[244:247], v[52:55], v[120:123]\n\t"
    "v_mfma_f32_16x16x32_bf16 v[124:127], a[224:227], v[44:47], v[124:127]\n\t"
    "ds_read_b128 v[76:79], v36 offset:6272\n\t"
    "v_mfma_f32_16x16x32_bf16 v[124:127], a[236:239], v[48:51], v[124:127]\n\t"
    "v_mfma_f32_16x16x32_bf16 v[124:127], a[248:251], v[52:55], v[124:127]\n\t"
    "; ---- unit 73: row 6 j 0 b 1 ds [2]\n\t"
    "s_waitcnt lgkmcnt(3)\n\t"
    "v_mfma_f32_16x16x32_bf16 v[128:131], a[216:219], v[56:59], v[128:131]\n\t"
    "v_add_u32 v37, s51, v12\n\t"
    "v_mfma_f32_16x16x32_bf16 v[128:131], a[228:231], v[60:63], v[128:131]\n\t"
    "v_mfma_f32_16x16x32_bf16 v[128:131], a[240:243], v[64:67], v[128:131]\n\t"
    "ds_read_b128 v[44:47], v37 offset:0\n\t"
    "v_mfma_f32_16x16x32_bf16 v[132:135], a[220:223], v[56:59], v[132:135]\n\t"
    "v_mfma_f32_16x16x32_bf16 v[132:135], a[232:235], v[60:63], v[132:135]\n\t"
    "ds_read_b128 v[48:51], v37 offset:64\n\t"
    "v_mfma_f32_16x16x32_bf16 v[132:135], a[244:247], v[64:67], v[132:135]\n\t"
    "v_mfma_f32_16x16x32_bf16 v[136:139], a[224:227], v[56:59], v[136:139]\n\t"
    "ds_read_b128 v[52:55], v37 offset:128\n\t"
    "v_mfma_f32_16x16x32_bf16 v[136:139], a[236:239], v[60:63], v[136:139]\n\t"
    "v_mfma_f32_16x16x32_bf16 v[136:139], a[248:251], v[64:67], v[136:139]\n\t"
    "; ---- unit 74: row 6 j 0 b 2 ds [2]\n\t"
    "s_barrier\n\t"
    "s_waitcnt lgkmcnt(3)\n\t"
    "v_mfma_f32_16x16x32_bf16 v[140:143], a[216:219], v[68:71], v[140:143]\n\t"
    "ds_read_b128 v[56:59], v37 offset:3072\n\t"
    "v_mfma_f32_16x16x32_bf16 v[140:143], a[228:231], v[72:75], v[140:143]\n\t"
    "v_add_u32 v23, 40960, v18\n\t"
    "v_mfma_f32_16x16x32_bf16 v[140:143], a[240:243], v[76:79], v[140:143]\n\t"
    "s_add_u32 s42, %[oso], 442368\n\t"
    "v_mfma_f32_16x16x32_bf16 v[144:147], a[220:223], v[68:71], v[144:147]\n\t"
    "ds_read_b128 v[60:63], v37 offset:3136\n\t"
    "v_mfma_f32_16x16x32_bf16 v[144:147], a[232:235], v[72:75], v[144:147]\n\t"
    "ds_read_b128 v[24:27], v23 offset:0\n\t"
    "v_mfma_f32_16x16x32_bf16 v[144:147], a[244:247], v[76:79], v[144:147]\n\t"
    "ds_read_b128 v[28:31], v23 offset:1024\n\t"
    "v_mfma_f32_16x16x32_bf16 v[148:151], a[224:227], v[68:71], v[148:151]\n\t"
    "ds_read_b128 v[64:67], v37 offset:3200\n\t"
    "v_mfma_f32_16x16x32_bf16 v[148:151], a[236:239], v[72:75], v[148:151]\n\t"
    "v_mfma_f32_16x16x32_bf16 v[148:151], a[248:251], v[76:79], v[148:151]\n\t"
    "; ---- unit 75: row 6 j 1 b 0 ds [2]\n\t"
    "s_waitcnt lgkmcnt(5)\n\t"
    "v_mfma_f32_16x16x32_bf16 v[116:119], a[252:255], v[44:47], v[116:119]\n\t"
    "ds_read_b128 v[68:71], v37 offset:6144\n\t"
    "v_mfma_f32_16x16x32_bf16 v[116:119], v[196:199], v[48:51], v[116:119]\n\t"
    "v_mfma_f32_16x16x32_bf16 v[116:119], v[208:211], v[52:55], v[116:119]\n\t"
    "v_mfma_f32_16x16x32_bf16 v[120:123], v[188:191], v[44:47], v[120:123]\n\t"
    "ds_read_b128 v[72:75], v37 offset:6208\n\t"
    "v_mfma_f32_16x16x32_bf16 v[120:123], v[200:203], v[48:51], v[120:123]\n\t"
    "s_waitcnt lgkmcnt(4)\n\t"
    "buffer_store_dwordx4 v[24:27], v17, %[srdo], s42 offen offset:0 sc1\n\t"
    "v_mfma_f32_16x16x32_bf16 v[120:123], v[212:215], v[52:55], v[120:123]\n\t"
    "ds_read_b128 v[24:27], v23 offset:2048\n\t"
    "v_mfma_f32_16x16x32_bf16 v[124:127], v[192:195], v[44:47], v[124:127]\n\t"
    "ds_read_b128 v[76:79], v37 offset:6272\n\t"
    "v_mfma_f32_16x16x32_bf16 v[124:127], v[204:207], v[48:51], v[124:127]\n\t"
    "s_waitcnt lgkmcnt(5)\n\t"
    "buffer_store_dwordx4 v[28:31], v17, %[srdo], s42 offen offset:1024 sc1\n\t"
    "v_mfma_f32_16x16x32_bf16 v[124:127], v[216:219], v[52:55], v[124:127]\n\t"
    "ds_read_b128 v[28:31], v23 offset:3072\n\t"
    "; ---- unit 76: row 6 j 1 b 1 ds [2]\n\t"
    "s_waitcnt lgkmcnt(5)\n\t"
    "v_mfma_f32_16x16x32_bf16 v[128:131], a[252:255], v[56:59], v[128:131]\n\t"
    "v_add_u32 v34, s51, v13\n\t"
    "v_mfma_f32_16x16x32_bf16 v[128:131], v[196:199], v[60:63], v[128:131]\n\t"
    "v_mfma_f32_16x16x32_bf16 v[128:131], v[208:211], v[64:67], v[128:131]\n\t"
    "ds_read_b128 v[44:47], v34 offset:0\n\t"
    "v_mfma_f32_16x16x32_bf16 v[132:135], v[188:191], v[56:59], v[132:135]\n\t"
    "v_mfma_f32_16x16x32_bf16 v[132:135], v[200:203], v[60:63], v[132:135]\n\t"
    "ds_read_b128 v[48:51], v34 offset:64\n\t"
    "v_mfma_f32_16x16x32_bf16 v[132:135], v[212:215], v[64:67], v[132:135]\n\t"
    "v_mfma_f32_16x16x32_bf16 v[136:139], v[192:195], v[56:59], v[136:139]\n\t"
    "ds_read_b128 v[52:55], v34 offset:128\n\t"
    "v_mfma_f32_16x16x32_bf16 v[136:139], v[204:207], v[60:63], v[136:139]\n\t"
    "v_mfma_f32_16x16x32_bf16 v[136:139], v[216:219], v[64:67], v[136:139]\n\t"
    "s_waitcnt lgkmcnt(5)\n\t"
    "buffer_store_dwordx4 v[24:27], v17, %[srdo], s42 offen offset:2048 sc1\n\t"
    "; ---- unit 77: row 6 j 1 b 2 ds [2]\n\t"
    "s_waitcnt lgkmcnt(4)\n\t"
    "v_mfma_f32_16x16x32_bf16 v[140:143], a[252:255], v[68:71], v[140:143]\n\t"
    "ds_read_b128 v[56:59], v34 offset:3072\n\t"
    "v_mfma_f32_16x16x32_bf16 v[140:143], v[196:199], v[72:75], v[140:143]\n\t"
    "ds_read_b128 v[24:27], v23 offset:4096\n\t"
    "v_mfma_f32_16x16x32_bf16 v[140:143], v[208:211], v[76:79], v[140:143]\n\t"
    "s_waitcnt lgkmcnt(5)\n\t"
    "buffer_store_dwordx4 v[28:31], v17, %[srdo], s42 offen offset:3072 sc1\n\t"
    "v_mfma_f32_16x16x32_bf16 v[144:147], v[188:191], v[68:71], v[144:147]\n\t"
    "ds_read_b128 v[60:63], v34 offset:3136\n\t"
    "v_mfma_f32_16x16x32_bf16 v[144:147], v[200:203], v[72:75], v[144:147]\n\t"
    "v_mfma_f32_16x16x32_bf16 v[144:147], v[212:215], v[76:79], v[144:147]\n\t"
    "v_mfma_f32_16x16x32_bf16 v[148:151], v[192:195], v[68:71], v[148:151]\n\t"
    "ds_read_b128 v[64:67], v34 offset:3200\n\t"
    "v_mfma_f32_16x16x32_bf16 v[148:151], v[204:207], v[72:75], v[148:151]\n\t"
    "v_mfma_f32_16x16x32_bf16 v[148:151], v[216:219], v[76:79], v[148:151]\n\t"
    "; ---- unit 78: row 6 j 2 b 0 ds [2]\n\t"
    "s_waitcnt lgkmcnt(4)\n\t"
    "v_mfma_f32_16x16x32_bf16 v[116:119], v[220:223], v[44:47], v[116:119]\n\t"
    "ds_read_b128 v[68:71], v34 offset:6144\n\t"
    "v_mfma_f32_16x16x32_bf16 v[116:119], v[232:235], v[48:51], v[116:119]\n\t"
    "v_mfma_f32_16x16x32_bf16 v[116:119], v[244:247], v[52:55], v[116:119]\n\t"
    "s_waitcnt lgkmcnt(3)\n\t"
    "buffer_store_dwordx4 v[24:27], v16, %[srdo], s42 offen sc1\n\t"
    "v_mfma_f32_16x16x32_bf16 v[120:123], v[224:227], v[44:47], v[120:123]\n\t"
    "ds_read_b128 v[72:75], v34 offset:6208\n\t"
    "v_mfma_f32_16x16x32_bf16 v[120:123], v[236:239], v[48:51], v[120:123]\n\t"
    "v_mfma_f32_16x16x32_bf16 v[120:123], v[248:251], v[52:55], v[120:123]\n\t"
    "v_mfma_f32_16x16x32_bf16 v[124:127], v[228:231], v[44:47], v[124:127]\n\t"
    "ds_read_b128 v[76:79], v34 offset:6272\n\t"
    "v_mfma_f32_16x16x32_bf16 v[124:127], v[240:243], v[48:51], v[124:127]\n\t"
    "v_mfma_f32_16x16x32_bf16 v[124:127], v[252:255], v[52:55], v[124:127]\n\t"
    "; ---- unit 79: row 6 j 2 b 1 ds [2]\n\t"
    "s_waitcnt lgkmcnt(3)\n\t"
    "v_mfma_f32_16x16x32_bf16 v[128:131], v[220:223], v[56:59], v[128:131]\n\t"
    "ds_read_b128 v[44:47], v36 offset:0\n\t"
    "v_mfma_f32_16x16x32_bf16 v[128:131], v[232:235], v[60:63], v[128:131]\n\t"
    "v_add_u32 v32, 122880, v20\n\t"
    "v_mfma_f32_16x16x32_bf16 v[128:131], v[244:247], v[64:67], v[128:131]\n\t"
    "v_add_u32 v33, 122880, v19\n\t"
    "v_mfma_f32_16x16x32_bf16 v[132:135], v[224:227], v[56:59], v[132:135]\n\t"
    "ds_read_b128 v[48:51], v36 offset:64\n\t"
    "v_mfma_f32_16x16x32_bf16 v[132:135], v[236:239], v[60:63], v[132:135]\n\t"
    "v_cvt_pk_bf16_f32 v38, v116, v117\n\t"
    "v_mfma_f32_16x16x32_bf16 v[132:135], v[248:251], v[64:67], v[132:135]\n\t"
    "v_cvt_pk_bf16_f32 v39, v118, v119\n\t"
    "v_mfma_f32_16x16x32_bf16 v[136:139], v[228:231], v[56:59], v[136:139]\n\t"
    "ds_read_b128 v[52:55], v36 offset:128\n\t"
    "v_mfma_f32_16x16x32_bf16 v[136:139], v[240:243], v[60:63], v[136:139]\n\t"
    "v_cvt_pk_bf16_f32 v40, v120, v121\n\t"
    "v_mfma_f32_16x16x32_bf16 v[136:139], v[252:255], v[64:67], v[136:139]\n\t"
    "v_cvt_pk_bf16_f32 v41, v122, v123\n\t"
    "; ---- unit 80: row 6 j 2 b 2 ds [2]\n\t"
    "s_waitcnt lgkmcnt(3)\n\t"
    "v_mfma_f32_16x16x32_bf16 v[140:143], v[220:223], v[68:71], v[140:143]\n\t"
    "v_add_u32 v35, s52, v11\n\t"
    "v_mfma_f32_16x16x32_bf16 v[140:143], v[232:235], v[72:75], v[140:143]\n\t"
    "ds_write_b128 v32, v[38:41] offset:0\n\t"
    "v_mfma_f32_16x16x32_bf16 v[140:143], v[244:247], v[76:79], v[140:143]\n\t"
    "ds_read_b128 v[56:59], v35 offset:0\n\t"
    "v_mfma_f32_16x16x32_bf16 v[144:147], v[224:227], v[68:71], v[144:147]\n\t"
    "v_cvt_pk_bf16_f32 v42, v124, v125\n\t"
    "v_mfma_f32_16x16x32_bf16 v[144:147], v[236:239], v[72:75], v[144:147]\n\t"
    "ds_read_b128 v[60:63], v35 offset:64\n\t"
    "v_mfma_f32_16x16x32_bf16 v[144:147], v[248:251], v[76:79], v[144:147]\n\t"
    "v_cvt_pk_bf16_f32 v43, v126, v127\n\t"
    "v_mfma_f32_16x16x32_bf16 v[148:151], v[228:231], v[68:71], v[148:151]\n\t"
    "ds_read_b128 v[64:67], v35 offset:128\n\t"
    "v_mfma_f32_16x16x32_bf16 v[148:151], v[240:243], v[72:75], v[148:151]\n\t"
    "ds_write_b64 v33, v[42:43] offset:0\n\t"
    "v_mfma_f32_16x16x32_bf16 v[148:151], v[252:255], v[76:79], v[148:151]\n\t"
    "s_nop 1\n\t"
    "v_cvt_pk_bf16_f32 v38, v128, v129\n\t"
    "; ---- unit 81: row 6 j 0 b 0 ds [1]\n\t"
    "s_waitcnt lgkmcnt(5)\n\t"
    "v_mfma_f32_16x16x32_bf16 v[152:155], a[108:111], v[44:47], v[152:155]\n\t"
    "ds_read_b128 v[68:71], v37 offset:0\n\t"
    "v_mfma_f32_16x16x32_bf16 v[152:155], a[120:123], v[48:51], v[152:155]\n\t"
    "v_cvt_pk_bf16_f32 v39, v130, v131\n\t"
    "v_mfma_f32_16x16x32_bf16 v[152:155], a[132:135], v[52:55], v[152:155]\n\t"
    "v_cvt_pk_bf16_f32 v40, v132, v133\n\t"
    "v_mfma_f32_16x16x32_bf16 v[156:159], a[112:115], v[44:47], v[156:159]\n\t"
    "ds_read_b128 v[72:75], v37 offset:64\n\t"
    "v_mfma_f32_16x16x32_bf16 v[156:159], a[124:127], v[48:51], v[156:159]\n\t"
    "v_cvt_pk_bf16_f32 v41, v134, v135\n\t"
    "v_mfma_f32_16x16x32_bf16 v[156:159], a[136:139], v[52:55], v[156:159]\n\t"
    "ds_write_b128 v32, v[38:41] offset:3072\n\t"
    "v_mfma_f32_16x16x32_bf16 v[160:163], a[116:119], v[44:47], v[160:163]\n\t"
    "ds_read_b128 v[76:79], v37 offset:128\n\t"
    "v_mfma_f32_16x16x32_bf16 v[160:163], a[128:131], v[48:51], v[160:163]\n\t"
    "v_cvt_pk_bf16_f32 v42, v136, v137\n\t"
    "v_mfma_f32_16x16x32_bf16 v[160:163], a[140:143], v[52:55], v[160:163]\n\t"
    "v_cvt_pk_bf16_f32 v43, v138, v139\n\t"
    "; ---- unit 82: row 7 j 0 b 0 ds [2]\n\t"
    "s_waitcnt lgkmcnt(5)\n\t"
    "v_mfma_f32_16x16x32_bf16 v[152:155], a[216:219], v[56:59], v[152:155]\n\t"
    "v_add_u32 v34, s52, v12\n\t"
    "v_mfma_f32_16x16x32_bf16 v[152:155], a[228:231], v[60:63], v[152:155]\n\t"
    "ds_write_b64 v33, v[42:43] offset:3072\n\t"
    "v_mfma_f32_16x16x32_bf16 v[152:155], a[240:243], v[64:67], v[152:155]\n\t"
    "ds_read_b128 v[44:47], v34 offset:0\n\t"
    "v_mfma_f32_16x16x32_bf16 v[156:159], a[220:223], v[56:59], v[156:159]\n\t"
    "v_cvt_pk_bf16_f32 v38, v140, v141\n\t"
    "v_mfma_f32_16x16x32_bf16 v[156:159], a[232:235], v[60:63], v[156:159]\n\t"
    "ds_read_b128 v[48:51], v34 offset:64\n\t"
    "v_mfma_f32_16x16x32_bf16 v[156:159], a[244:247], v[64:67], v[156:159]\n\t"
    "v_cvt_pk_bf16_f32 v39, v142, v143\n\t"
    "v_mfma_f32_16x16x32_bf16 v[160:163], a[224:227], v[56:59], v[160:163]\n\t"
    "ds_read_b128 v[52:55], v34 offset:128\n\t"
    "v_mfma_f32_16x16x32_bf16 v[160:163], a[236:239], v[60:63], v[160:163]\n\t"
    "v_cvt_pk_bf16_f32 v40, v144, v145\n\t"
    "v_mfma_f32_16x16x32_bf16 v[160:163], a[248:251], v[64:67], v[160:163]\n\t"
    "v_cvt_pk_bf16_f32 v41, v146, v147\n\t"
    "; ---- unit 83: row 6 j 1 b 0 ds [1]\n\t"
    "s_waitcnt lgkmcnt(4)\n\t"
    "v_mfma_f32_16x16x32_bf16 v[152:155], a[144:147], v[68:71], v[152:155]\n\t"
    "v_add_u32 v36, s51, v13\n\t"
    "v_mfma_f32_16x16x32_bf16 v[152:155], a[156:159], v[72:75], v[152:155]\n\t"
    "ds_write_b128 v32, v[38:41] offset:6144\n\t"
    "v_mfma_f32_16x16x32_bf16 v[152:155], a[168:171], v[76:79], v[152:155]\n\t"
    "ds_read_b128 v[56:59], v36 offset:0\n\t"
    "v_mfma_f32_16x16x32_bf16 v[156:159], a[148:151], v[68:71], v[156:159]\n\t"
    "v_cvt_pk_bf16_f32 v42, v148, v149\n\t"
    "v_mfma_f32_16x16x32_bf16 v[156:159], a[160:163], v[72:75], v[156:159]\n\t"
    "ds_read_b128 v[60:63], v36 offset:64\n\t"
    "v_mfma_f32_16x16x32_bf16 v[156:159], a[172:175], v[76:79], v[156:159]\n\t"
    "v_cvt_pk_bf16_f32 v43, v150, v151\n\t"
    "v_mfma_f32_16x16x32_bf16 v[160:163], a[152:155], v[68:71], v[160:163]\n\t"
    "ds_read_b128 v[64:67], v36 offset:128\n\t"
    "v_mfma_f32_16x16x32_bf16 v[160:163], a[164:167], v[72:75], v[160:163]\n\t"
    "ds_write_b64 v33, v[42:43] offset:6144\n\t"
    "v_mfma_f32_16x16x32_bf16 v[160:163], a[176:179], v[76:79], v[160:163]\n\t"
    "; ---- unit 84: row 7 j 1 b 0 ds [2]\n\t"
    "s_waitcnt lgkmcnt(5)\n\t"
    "v_mfma_f32_16x16x32_bf16 v[152:155], a[252:255], v[44:47], v[152:155]\n\t"
    "v_add_u32 v35, s52, v13\n\t"
    "v_mfma_f32_16x16x32_bf16 v[152:155], v[196:199], v[48:51], v[152:155]\n\t"
    "v_mfma_f32_16x16x32_bf16 v[152:155], v[208:211], v[52:55], v[152:155]\n\t"
    "ds_read_b128 v[68:71], v35 offset:0\n\t"
    "v_mfma_f32_16x16x32_bf16 v[156:159], v[188:191], v[44:47], v[156:159]\n\t"
    "v_mfma_f32_16x16x32_bf16 v[156:159], v[200:203], v[48:51], v[156:159]\n\t"
    "ds_read_b128 v[72:75], v35 offset:64\n\t"
    "v_mfma_f32_16x16x32_bf16 v[156:159], v[212:215], v[52:55], v[156:159]\n\t"
    "v_mfma_f32_16x16x32_bf16 v[160:163], v[192:195], v[44:47], v[160:163]\n\t"
    "ds_read_b128 v[76:79], v35 offset:128\n\t"
    "v_mfma_f32_16x16x32_bf16 v[160:163], v[204:207], v[48:51], v[160:163]\n\t"
    "v_mfma_f32_16x16x32_bf16 v[160:163], v[216:219], v[52:55], v[160:163]\n\t"
    "; ---- unit 85: row 6 j 2 b 0 ds [1]\n\t"
    "s_waitcnt lgkmcnt(4)\n\t"
    "v_mfma_f32_16x16x32_bf16 v[152:155], a[180:183], v[56:59], v[152:155]\n\t"
    "v_add_u32 v37, s51, v11\n\t"
    "v_mfma_f32_16x16x32_bf16 v[152:155], a[192:195], v[60:63], v[152:155]\n\t"
    "v_mfma_f32_16x16x32_bf16 v[152:155], a[204:207], v[64:67], v[152:155]\n\t"
    "ds_read_b128 v[44:47], v37 offset:3072\n\t"
    "v_mfma_f32_16x16x32_bf16 v[156:159], a[184:187], v[56:59], v[156:159]\n\t"
    "v_mfma_f32_16x16x32_bf16 v[156:159], a[196:199], v[60:63], v[156:159]\n\t"
    "ds_read_b128 v[48:51], v37 offset:3136\n\t"
    "v_mfma_f32_16x16x32_bf16 v[156:159], a[208:211], v[64:67], v[156:159]\n\t"
    "v_mfma_f32_16x16x32_bf16 v[160:163], a[188:191], v[56:59], v[160:163]\n\t"
    "ds_read_b128 v[52:55], v37 offset:3200\n\t"
    "v_mfma_f32_16x16x32_bf16 v[160:163], a[200:203], v[60:63], v[160:163]\n\t"
    "v_mfma_f32_16x16x32_bf16 v[160:163], a[212:215], v[64:67], v[160:163]\n\t"
    "; ---- unit 86: row 7 j 2 b 0 ds [2]\n\t"
    "s_waitcnt lgkmcnt(6)\n\t"
    "s_barrier\n\t"
    "s_waitcnt lgkmcnt(3)\n\t"
    "v_mfma_f32_16x16x32_bf16 v[152:155], v[220:223], v[68:71], v[152:155]\n\t"
    "v_add_u32 v34, s52, v11\n\t"
    "v_mfma_f32_16x16x32_bf16 v[152:155], v[232:235], v[72:75], v[152:155]\n\t"
    "v_add_u32 v23, 122880, v18\n\t"
    "v_mfma_f32_16x16x32_bf16 v[152:155], v[244:247], v[76:79], v[152:155]\n\t"
    "ds_read_b128 v[56:59], v34 offset:3072\n\t"
    "v_mfma_f32_16x16x32_bf16 v[156:159], v[224:227], v[68:71], v[156:159]\n\t"
    "s_add_u32 s42, %[oso], 589824\n\t"
    "v_mfma_f32_16x16x32_bf16 v[156:159], v[236:239], v[72:75], v[156:159]\n\t"
    "ds_read_b128 v[60:63], v34 offset:3136\n\t"
    "v_mfma_f32_16x16x32_bf16 v[156:159], v[248:251], v[76:79], v[156:159]\n\t"
    "ds_read_b128 v[24:27], v23 offset:0\n\t"
    "v_mfma_f32_16x16x32_bf16 v[160:163], v[228:231], v[68:71], v[160:163]\n\t"
    "ds_read_b128 v[64:67], v34 offset:3200\n\t"
    "v_mfma_f32_16x16x32_bf16 v[160:163], v[240:243], v[72:75], v[160:163]\n\t"
    "ds_read_b128 v[28:31], v23 offset:1024\n\t"
    "v_mfma_f32_16x16x32_bf16 v[160:163], v[252:255], v[76:79], v[160:163]\n\t"
    "; ---- unit 87: row 6 j 0 b 1 ds [1]\n\t"
    "s_waitcnt lgkmcnt(5)\n\t"
    "v_mfma_f32_16x16x32_bf16 v[164:167], a[108:111], v[44:47], v[164:167]\n\t"
    "v_add_u32 v36, s51, v12\n\t"
    "v_mfma_f32_16x16x32_bf16 v[164:167], a[120:123], v[48:51], v[164:167]\n\t"
    "v_mfma_f32_16x16x32_bf16 v[164:167], a[132:135], v[52:55], v[164:167]\n\t"
    "ds_read_b128 v[68:71], v36 offset:3072\n\t"
    "v_mfma_f32_16x16x32_bf16 v[168:171], a[112:115], v[44:47], v[168:171]\n\t"
    "v_mfma_f32_16x16x32_bf16 v[168:171], a[124:127], v[48:51], v[168:171]\n\t"
    "ds_read_b128 v[72:75], v36 offset:3136\n\t"
    "v_mfma_f32_16x16x32_bf16 v[168:171], a[136:139], v[52:55], v[168:171]\n\t"
    "v_mfma_f32_16x16x32_bf16 v[172:175], a[116:119], v[44:47], v[172:175]\n\t"
    "ds_read_b128 v[76:79], v36 offset:3200\n\t"
    "v_mfma_f32_16x16x32_bf16 v[172:175], a[128:131], v[48:51], v[172:175]\n\t"
    "s_waitcnt lgkmcnt(5)\n\t"
    "buffer_store_dwordx4 v[24:27], v17, %[srdo], s42 offen offset:0 sc1\n\t"
    "v_mfma_f32_16x16x32_bf16 v[172:175], a[140:143], v[52:55], v[172:175]\n\t"
    "ds_read_b128 v[24:27], v23 offset:2048\n\t"
    "; ---- unit 88: row 7 j 0 b 1 ds [2]\n\t"
    "s_waitcnt lgkmcnt(5)\n\t"
    "v_mfma_f32_16x16x32_bf16 v[164:167], a[216:219], v[56:59], v[164:167]\n\t"
    "v_add_u32 v35, s52, v12\n\t"
    "v_mfma_f32_16x16x32_bf16 v[164:167], a[228:231], v[60:63], v[164:167]\n\t"
    "s_waitcnt lgkmcnt(4)\n\t"
    "buffer_store_dwordx4 v[28:31], v17, %[srdo], s42 offen offset:1024 sc1\n\t"
    "v_mfma_f32_16x16x32_bf16 v[164:167], a[240:243], v[64:67], v[164:167]\n\t"
    "ds_read_b128 v[44:47], v35 offset:3072\n\t"
    "v_mfma_f32_16x16x32_bf16 v[168:171], a[220:223], v[56:59], v[168:171]\n\t"
    "ds_read_b128 v[28:31], v23 offset:3072\n\t"
    "v_mfma_f32_16x16x32_bf16 v[168:171], a[232:235], v[60:63], v[168:171]\n\t"
    "ds_read_b128 v[48:51], v35 offset:3136\n\t"
    "v_mfma_f32_16x16x32_bf16 v[168:171], a[244:247], v[64:67], v[168:171]\n\t"
    "v_mfma_f32_16x16x32_bf16 v[172:175], a[224:227], v[56:59], v[172:175]\n\t"
    "ds_read_b128 v[52:55], v35 offset:3200\n\t"
    "v_mfma_f32_16x16x32_bf16 v[172:175], a[236:239], v[60:63], v[172:175]\n\t"
    "v_mfma_f32_16x16x32_bf16 v[172:175], a[248:251], v[64:67], v[172:175]\n\t"
    "; ---- unit 89: row 6 j 1 b 1 ds [1]\n\t"
    "s_waitcnt lgkmcnt(5)\n\t"
    "v_mfma_f32_16x16x32_bf16 v[164:167], a[144:147], v[68:71], v[164:167]\n\t"
    "v_add_u32 v37, s51, v13\n\t"
    "v_mfma_f32_16x16x32_bf16 v[164:167], a[156:159], v[72:75], v[164:167]\n\t"
    "v_mfma_f32_16x16x32_bf16 v[164:167], a[168:171], v[76:79], v[164:167]\n\t"
    "ds_read_b128 v[56:59], v37 offset:3072\n\t"
    "v_mfma_f32_16x16x32_bf16 v[168:171], a[148:151], v[68:71], v[168:171]\n\t"
    "s_waitcnt lgkmcnt(5)\n\t"
    "buffer_store_dwordx4 v[24:27], v17, %[srdo], s42 offen offset:2048 sc1\n\t"
    "v_mfma_f32_16x16x32_bf16 v[168:171], a[160:163], v[72:75], v[168:171]\n\t"
    "ds_read_b128 v[60:63], v37 offset:3136\n\t"
    "v_mfma_f32_16x16x32_bf16 v[168:171], a[172:175], v[76:79], v[168:171]\n\t"
    "ds_read_b128 v[24:27], v23 offset:4096\n\t"
    "v_mfma_f32_16x16x32_bf16 v[172:175], a[152:155], v[68:71], v[172:175]\n\t"
    "ds_read_b128 v[64:67], v37 offset:3200\n\t"
    "v_mfma_f32_16x16x32_bf16 v[172:175], a[164:167], v[72:75], v[172:175]\n\t"
    "s_waitcnt lgkmcnt(6)\n\t"
    "buffer_store_dwordx4 v[28:31], v17, %[srdo], s42 offen offset:3072 sc1\n\t"
    "v_mfma_f32_16x16x32_bf16 v[172:175], a[176:179], v[76:79], v[172:175]\n\t"
    "; ---- unit 90: row 7 j 1 b 1 ds [2]\n\t"
    "s_waitcnt lgkmcnt(4)\n\t"
    "v_mfma_f32_16x16x32_bf16 v[164:167], a[252:255], v[44:47], v[164:167]\n\t"
    "v_add_u32 v34, s52, v13\n\t"
    "v_mfma_f32_16x16x32_bf16 v[164:167], v[196:199], v[48:51], v[164:167]\n\t"
    "v_mfma_f32_16x16x32_bf16 v[164:167], v[208:211], v[52:55], v[164:167]\n\t"
    "ds_read_b128 v[68:71], v34 offset:3072\n\t"
    "v_mfma_f32_16x16x32_bf16 v[168:171], v[188:191], v[44:47], v[168:171]\n\t"
    "v_mfma_f32_16x16x32_bf16 v[168:171], v[200:203], v[48:51], v[168:171]\n\t"
    "ds_read_b128 v[72:75], v34 offset:3136\n\t"
    "v_mfma_f32_16x16x32_bf16 v[168:171], v[212:215], v[52:55], v[168:171]\n\t"
    "v_mfma_f32_16x16x32_bf16 v[172:175], v[192:195], v[44:47], v[172:175]\n\t"
    "ds_read_b128 v[76:79], v34 offset:3200\n\t"
    "v_mfma_f32_16x16x32_bf16 v[172:175], v[204:207], v[48:51], v[172:175]\n\t"
    "v_mfma_f32_16x16x32_bf16 v[172:175], v[216:219], v[52:55], v[172:175]\n\t"
    "s_waitcnt lgkmcnt(4)\n\t"
    "buffer_store_dwordx4 v[24:27], v16, %[srdo], s42 offen sc1\n\t"
    "; ---- unit 91: row 6 j 2 b 1 ds [1]\n\t"
    "s_waitcnt lgkmcnt(3)\n\t"
    "v_mfma_f32_16x16x32_bf16 v[164:167], a[180:183], v[56:59], v[164:167]\n\t"
    "v_add_u32 v36, s51, v11\n\t"
    "v_mfma_f32_16x16x32_bf16 v[164:167], a[192:195], v[60:63], v[164:167]\n\t"
    "s_add_u32 s43, %[oso], 737280\n\t"
    "v_mfma_f32_16x16x32_bf16 v[164:167], a[204:207], v[64:67], v[164:167]\n\t"
    "ds_read_b128 v[44:47], v36 offset:6144\n\t"
    "v_mfma_f32_16x16x32_bf16 v[168:171], a[184:187], v[56:59], v[168:171]\n\t"
    "v_cvt_pk_bf16_f32 v38, v152, v153\n\t"
    "v_mfma_f32_16x16x32_bf16 v[168:171], a[196:199], v[60:63], v[168:171]\n\t"
    "ds_read_b128 v[48:51], v36 offset:6208\n\t"
    "v_mfma_f32_16x16x32_bf16 v[168:171], a[208:211], v[64:67], v[168:171]\n\t"
    "v_cvt_pk_bf16_f32 v39, v154, v155\n\t"
    "v_mfma_f32_16x16x32_bf16 v[172:175], a[188:191], v[56:59], v[172:175]\n\t"
    "ds_read_b128 v[52:55], v36 offset:6272\n\t"
    "v_mfma_f32_16x16x32_bf16 v[172:175], a[200:203], v[60:63], v[172:175]\n\t"
    "v_cvt_pk_bf16_f32 v40, v156, v157\n\t"
    "v_mfma_f32_16x16x32_bf16 v[172:175], a[212:215], v[64:67], v[172:175]\n\t"
    "v_cvt_pk_bf16_f32 v41, v158, v159\n\t"
    "; ---- unit 92: row 7 j 2 b 1 ds [2]\n\t"
    "s_waitcnt lgkmcnt(3)\n\t"
    "v_mfma_f32_16x16x32_bf16 v[164:167], v[220:223], v[68:71], v[164:167]\n\t"
    "v_add_u32 v35, s52, v11\n\t"
    "v_mfma_f32_16x16x32_bf16 v[164:167], v[232:235], v[72:75], v[164:167]\n\t"
    "buffer_store_dwordx4 v[38:41], v22, %[srdo], s43 offen offset:0\n\t"
    "v_mfma_f32_16x16x32_bf16 v[164:167], v[244:247], v[76:79], v[164:167]\n\t"
    "ds_read_b128 v[56:59], v35 offset:6144\n\t"
    "v_mfma_f32_16x16x32_bf16 v[168:171], v[224:227], v[68:71], v[168:171]\n\t"
    "v_cvt_pk_bf16_f32 v42, v160, v161\n\t"
    "v_mfma_f32_16x16x32_bf16 v[168:171], v[236:239], v[72:75], v[168:171]\n\t"
    "ds_read_b128 v[60:63], v35 offset:6208\n\t"
    "v_mfma_f32_16x16x32_bf16 v[168:171], v[248:251], v[76:79], v[168:171]\n\t"
    "v_cvt_pk_bf16_f32 v43, v162, v163\n\t"
    "v_mfma_f32_16x16x32_bf16 v[172:175], v[228:231], v[68:71], v[172:175]\n\t"
    "ds_read_b128 v[64:67], v35 offset:6272\n\t"
    "v_mfma_f32_16x16x32_bf16 v[172:175], v[240:243], v[72:75], v[172:175]\n\t"
    "buffer_store_dwordx2 v[42:43], v21, %[srdo], s43 offen offset:0\n\t"
    "v_mfma_f32_16x16x32_bf16 v[172:175], v[252:255], v[76:79], v[172:175]\n\t"
    "; ---- unit 93: row 6 j 0 b 2 ds [1]\n\t"
    "s_waitcnt lgkmcnt(3)\n\t"
    "v_mfma_f32_16x16x32_bf16 v[176:179], a[108:111], v[44:47], v[176:179]\n\t"
    "v_add_u32 v37, s51, v12\n\t"
    "v_mfma_f32_16x16x32_bf16 v[176:179], a[120:123], v[48:51], v[176:179]\n\t"
    "s_add_u32 s43, %[oso], 737280\n\t"
    "v_mfma_f32_16x16x32_bf16 v[176:179], a[132:135], v[52:55], v[176:179]\n\t"
    "ds_read_b128 v[68:71], v37 offset:6144\n\t"
    "v_mfma_f32_16x16x32_bf16 v[180:183], a[112:115], v[44:47], v[180:183]\n\t"
    "v_cvt_pk_bf16_f32 v38, v164, v165\n\t"
    "v_mfma_f32_16x16x32_bf16 v[180:183], a[124:127], v[48:51], v[180:183]\n\t"
    "ds_read_b128 v[72:75], v37 offset:6208\n\t"
    "v_mfma_f32_16x16x32_bf16 v[180:183], a[136:139], v[52:55], v[180:183]\n\t"
    "v_cvt_pk_bf16_f32 v39, v166, v167\n\t"
    "v_mfma_f32_16x16x32_bf16 v[184:187], a[116:119], v[44:47], v[184:187]\n\t"
    "ds_read_b128 v[76:79], v37 offset:6272\n\t"
    "v_mfma_f32_16x16x32_bf16 v[184:187], a[128:131], v[48:51], v[184:187]\n\t"
    "v_cvt_pk_bf16_f32 v40, v168, v169\n\t"
    "v_mfma_f32_16x16x32_bf16 v[184:187], a[140:143], v[52:55], v[184:187]\n\t"
    "v_cvt_pk_bf16_f32 v41, v170, v171\n\t"
    "; ---- unit 94: row 7 j 0 b 2 ds [2]\n\t"
    "s_waitcnt lgkmcnt(3)\n\t"
    "v_mfma_f32_16x16x32_bf16 v[176:179], a[216:219], v[56:59], v[176:179]\n\t"
    "v_add_u32 v34, s52, v12\n\t"
    "v_mfma_f32_16x16x32_bf16 v[176:179], a[228:231], v[60:63], v[176:179]\n\t"
    "buffer_store_dwordx4 v[38:41], v22, %[srdo], s43 offen offset:3072\n\t"
    "v_mfma_f32_16x16x32_bf16 v[176:179], a[240:243], v[64:67], v[176:179]\n\t"
    "ds_read_b128 v[44:47], v34 offset:6144\n\t"
    "v_mfma_f32_16x16x32_bf16 v[180:183], a[220:223], v[56:59], v[180:183]\n\t"
    "v_cvt_pk_bf16_f32 v42, v172, v173\n\t"
    "v_mfma_f32_16x16x32_bf16 v[180:183], a[232:235], v[60:63], v[180:183]\n\t"
    "ds_read_b128 v[48:51], v34 offset:6208\n\t"
    "v_mfma_f32_16x16x32_bf16 v[180:183], a[244:247], v[64:67], v[180:183]\n\t"
    "v_cvt_pk_bf16_f32 v43, v174, v175\n\t"
    "v_mfma_f32_16x16x32_bf16 v[184:187], a[224:227], v[56:59], v[184:187]\n\t"
    "ds_read_b128 v[52:55], v34 offset:6272\n\t"
    "v_mfma_f32_16x16x32_bf16 v[184:187], a[236:239], v[60:63], v[184:187]\n\t"
    "buffer_store_dwordx2 v[42:43], v21, %[srdo], s43 offen offset:3072\n\t"
    "v_mfma_f32_16x16x32_bf16 v[184:187], a[248:251], v[64:67], v[184:187]\n\t"
    "; ---- unit 95: row 6 j 1 b 2 ds [1]\n\t"
    "s_waitcnt lgkmcnt(3)\n\t"
    "v_mfma_f32_16x16x32_bf16 v[176:179], a[144:147], v[68:71], v[176:179]\n\t"
    "v_add_u32 v36, s51, v13\n\t"
    "v_mfma_f32_16x16x32_bf16 v[176:179], a[156:159], v[72:75], v[176:179]\n\t"
    "v_mfma_f32_16x16x32_bf16 v[176:179], a[168:171], v[76:79], v[176:179]\n\t"
    "ds_read_b128 v[56:59], v36 offset:6144\n\t"
    "v_mfma_f32_16x16x32_bf16 v[180:183], a[148:151], v[68:71], v[180:183]\n\t"
    "v_mfma_f32_16x16x32_bf16 v[180:183], a[160:163], v[72:75], v[180:183]\n\t"
    "ds_read_b128 v[60:63], v36 offset:6208\n\t"
    "v_mfma_f32_16x16x32_bf16 v[180:183], a[172:175], v[76:79], v[180:183]\n\t"
    "v_mfma_f32_16x16x32_bf16 v[184:187], a[152:155], v[68:71], v[184:187]\n\t"
    "ds_read_b128 v[64:67], v36 offset:6272\n\t"
    "v_mfma_f32_16x16x32_bf16 v[184:187], a[164:167], v[72:75], v[184:187]\n\t"
    "v_mfma_f32_16x16x32_bf16 v[184:187], a[176:179], v[76:79], v[184:187]\n\t"
    "; ---- unit 96: row 7 j 1 b 2 ds [2]\n\t"
    "s_waitcnt lgkmcnt(3)\n\t"
    "v_mfma_f32_16x16x32_bf16 v[176:179], a[252:255], v[44:47], v[176:179]\n\t"
    "v_add_u32 v35, s52, v13\n\t"
    "v_mfma_f32_16x16x32_bf16 v[176:179], v[196:199], v[48:51], v[176:179]\n\t"
    "v_mfma_f32_16x16x32_bf16 v[176:179], v[208:211], v[52:55], v[176:179]\n\t"
    "ds_read_b128 v[68:71], v35 offset:6144\n\t"
    "v_mfma_f32_16x16x32_bf16 v[180:183], v[188:191], v[44:47], v[180:183]\n\t"
    "v_mfma_f32_16x16x32_bf16 v[180:183], v[200:203], v[48:51], v[180:183]\n\t"
    "ds_read_b128 v[72:75], v35 offset:6208\n\t"
    "v_mfma_f32_16x16x32_bf16 v[180:183], v[212:215], v[52:55], v[180:183]\n\t"
    "v_mfma_f32_16x16x32_bf16 v[184:187], v[192:195], v[44:47], v[184:187]\n\t"
    "ds_read_b128 v[76:79], v35 offset:6272\n\t"
    "v_mfma_f32_16x16x32_bf16 v[184:187], v[204:207], v[48:51], v[184:187]\n\t"
    "v_mfma_f32_16x16x32_bf16 v[184:187], v[216:219], v[52:55], v[184:187]\n\t"
    "; ---- unit 97: row 6 j 2 b 2 ds [1]\n\t"
    "s_waitcnt lgkmcnt(3)\n\t"
    "v_mfma_f32_16x16x32_bf16 v[176:179], a[180:183], v[56:59], v[176:179]\n\t"
    "v_mfma_f32_16x16x32_bf16 v[176:179], a[192:195], v[60:63], v[176:179]\n\t"
    "v_mfma_f32_16x16x32_bf16 v[176:179], a[204:207], v[64:67], v[176:179]\n\t"
    "v_mfma_f32_16x16x32_bf16 v[180:183], a[184:187], v[56:59], v[180:183]\n\t"
    "v_mfma_f32_16x16x32_bf16 v[180:183], a[196:199], v[60:63], v[180:183]\n\t"
    "v_mfma_f32_16x16x32_bf16 v[180:183], a[208:211], v[64:67], v[180:183]\n\t"
    "v_mfma_f32_16x16x32_bf16 v[184:187], a[188:191], v[56:59], v[184:187]\n\t"
    "v_mfma_f32_16x16x32_bf16 v[184:187], a[200:203], v[60:63], v[184:187]\n\t"
    "v_mfma_f32_16x16x32_bf16 v[184:187], a[212:215], v[64:67], v[184:187]\n\t"
    "; ---- unit 98: row 7 j 2 b 2 ds [2]\n\t"
    "s_waitcnt lgkmcnt(0)\n\t"
    "v_mfma_f32_16x16x32_bf16 v[176:179], v[220:223], v[68:71], v[176:179]\n\t"
    "v_mfma_f32_16x16x32_bf16 v[176:179], v[232:235], v[72:75], v[176:179]\n\t"
    "v_mfma_f32_16x16x32_bf16 v[176:179], v[244:247], v[76:79], v[176:179]\n\t"
    "v_mfma_f32_16x16x32_bf16 v[180:183], v[224:227], v[68:71], v[180:183]\n\t"
    "v_mfma_f32_16x16x32_bf16 v[180:183], v[236:239], v[72:75], v[180:183]\n\t"
    "v_mfma_f32_16x16x32_bf16 v[180:183], v[248:251], v[76:79], v[180:183]\n\t"
    "v_mfma_f32_16x16x32_bf16 v[184:187], v[228:231], v[68:71], v[184:187]\n\t"
    "v_mfma_f32_16x16x32_bf16 v[184:187], v[240:243], v[72:75], v[184:187]\n\t"
    "v_mfma_f32_16x16x32_bf16 v[184:187], v[252:255], v[76:79], v[184:187]\n\t"
    "; ---- tail\n\t"
    "s_add_u32 s43, %[oso], 743424\n\t"
    "s_nop 2\n\t"
    "v_cvt_pk_bf16_f32 v38, v176, v177\n\t"
    "s_nop 0\n\t"
    "v_cvt_pk_bf16_f32 v39, v178, v179\n\t"
    "s_nop 1\n\t"
    "v_cvt_pk_bf16_f32 v40, v180, v181\n\t"
    "v_cvt_pk_bf16_f32 v41, v182, v183\n\t"
    "buffer_store_dwordx4 v[38:41], v22, %[srdo], s43 offen offset:0\n\t"
    "s_nop 0\n\t"
    "s_nop 1\n\t"
    "v_cvt_pk_bf16_f32 v42, v184, v185\n\t"
    "v_cvt_pk_bf16_f32 v43, v186, v187\n\t"
    "buffer_store_dwordx2 v[42:43], v21, %[srdo], s43 offen offset:0\n\t"
    ".Lconv_code_end:\n\t"
    :
    : [vwp] "v"(vwp), [vx0] "v"(vx0), [vx1] "v"(vx1), [vxp] "v"(vxp), [vx2] "v"(vx2), [vx3] "v"(vx3), [vx4] "v"(vx4),
      [vw0] "v"(vw0), [vw1] "v"(vw1), [vl16] "v"(vl16), [sbase] "s"(sbase), [nh] "s"(nh),
      [srdx] "s"(srdx), [srdw] "s"(srdw), [srdo] "s"(srdo),
      [xso] "s"(xso), [xsof] "s"(xsof), [xsol] "s"(xsol), [m0x] "s"(m0x), [m0w] "s"(m0w), [oso] "s"(oso),
      [mh] "s"(mh)
    : "v10", "v11", "v12", "v13", "v14", "v15", "v16", "v17", "v18", "v19", "v20", "v21", "v22", "v23", "v24", "v25", "v26", "v27", "v28", "v29", "v30", "v31", "v32", "v33", "v34", "v35", "v36", "v37", "v38", "v39", "v40", "v41", "v42", "v43", "v44", "v45", "v46", "v47", "v48", "v49", "v50", "v51", "v52", "v53", "v54", "v55", "v56", "v57", "v58", "v59", "v60", "v61", "v62", "v63", "v64", "v65", "v66", "v67", "v68", "v69", "v70", "v71", "v72", "v73", "v74", "v75", "v76", "v77", "v78", "v79", "v80", "v81", "v82", "v83", "v84", "v85", "v86", "v87", "v88", "v89", "v90", "v91", "v92", "v93", "v94", "v95", "v96", "v97", "v98", "v99", "v100", "v101", "v102", "v103", "v104", "v105", "v106", "v107", "v108", "v109", "v110", "v111", "v112", "v113", "v114", "v115", "v116", "v117", "v118", "v119", "v120", "v121", "v122", "v123", "v124", "v125", "v126", "v127", "v128", "v129", "v130", "v131", "v132", "v133", "v134", "v135", "v136", "v137", "v138", "v139", "v140", "v141", "v142", "v143", "v144", "v145", "v146", "v147", "v148", "v149", "v150", "v151", "v152", "v153", "v154", "v155", "v156", "v157", "v158", "v159", "v160", "v161", "v162", "v163", "v164", "v165", "v166", "v167", "v168", "v169", "v170", "v171", "v172", "v173", "v174", "v175", "v176", "v177", "v178", "v179", "v180", "v181", "v182", "v183", "v184", "v185", "v186", "v187", "v188", "v189", "v190", "v191", "v192", "v193", "v194", "v195", "v196", "v197", "v198", "v199", "v200", "v201", "v202", "v203", "v204", "v205", "v206", "v207", "v208", "v209", "v210", "v211", "v212", "v213", "v214", "v215", "v216", "v217", "v218", "v219", "v220", "v221", "v222", "v223", "v224", "v225", "v226", "v227", "v228", "v229", "v230", "v231", "v232", "v233", "v234", "v235", "v236", "v237", "v238", "v239", "v240", "v241", "v242", "v243", "v244", "v245", "v246", "v247", "v248", "v249", "v250", "v251", "v252", "v253", "v254", "v255", "a0", "a1", "a2", "a3", "a4", "a5", "a6", "a7", "a8", "a9", "a10", "a11", "a12", "a13", "a14", "a15", "a16", "a17", "a18", "a19", "a20", "a21", "a22", "a23", "a24", "a25", "a26", "a27", "a28", "a29", "a30", "a31", "a32", "a33", "a34", "a35", "a36", "a37", "a38", "a39", "a40", "a41", "a42", "a43", "a44", "a45", "a46", "a47", "a48", "a49", "a50", "a51", "a52", "a53", "a54", "a55", "a56", "a57", "a58", "a59", "a60", "a61", "a62", "a63", "a64", "a65", "a66", "a67", "a68", "a69", "a70", "a71", "a72", "a73", "a74", "a75", "a76", "a77", "a78", "a79", "a80", "a81", "a82", "a83", "a84", "a85", "a86", "a87", "a88", "a89", "a90", "a91", "a92", "a93", "a94", "a95", "a96", "a97", "a98", "a99", "a100", "a101", "a102", "a103", "a104", "a105", "a106", "a107", "a108", "a109", "a110", "a111", "a112", "a113", "a114", "a115", "a116", "a117", "a118", "a119", "a120", "a121", "a122", "a123", "a124", "a125", "a126", "a127", "a128", "a129", "a130", "a131", "a132", "a133", "a134", "a135", "a136", "a137", "a138", "a139", "a140", "a141", "a142", "a143", "a144", "a145", "a146", "a147", "a148", "a149", "a150", "a151", "a152", "a153", "a154", "a155", "a156", "a157", "a158", "a159", "a160", "a161", "a162", "a163", "a164", "a165", "a166", "a167", "a168", "a169", "a170", "a171", "a172", "a173", "a174", "a175", "a176", "a177", "a178", "a179", "a180", "a181", "a182", "a183", "a184", "a185", "a186", "a187", "a188", "a189", "a190", "a191", "a192", "a193", "a194", "a195", "a196", "a197", "a198", "a199", "a200", "a201", "a202", "a203", "a204", "a205", "a206", "a207", "a208", "a209", "a210", "a211", "a212", "a213", "a214", "a215", "a216", "a217", "a218", "a219", "a220", "a221", "a222", "a223", "a224", "a225", "a226", "a227", "a228", "a229", "a230", "a231", "a232", "a233", "a234", "a235", "a236", "a237", "a238", "a239", "a240", "a241", "a242", "a243", "a244", "a245", "a246", "a247", "a248", "a249", "a250", "a251", "a252", "a253", "a254", "a255", "s40", "s41", "s42", "s43", "s44", "s45", "s46", "s47", "s48", "s49", "s50", "s51", "s52", "s53", "s54", "s55", "s56", "s57", "s58", "s59", "s60", "s61", "s62", "s63", "s64", "s65", "s66", "s67", "s68", "s69", "s70", "s71", "s72", "s73", "s74", "s75", "s76", "s77", "s78", "s79", "s80", "s81", "s82", "s83", "s84", "s85", "s86", "s87", "s88", "s89", "s90", "s91", "s92", "s93", "s94", "s95", "s96", "s97", "s98", "s99", "scc", "memory");

}

extern "C" void kernel_launch(void* const* d_in, const int* in_sizes, int n_in,
                              void* d_out, int out_size, void* d_ws, size_t ws_size,
                              hipStream_t stream) {
  (void)in_sizes; (void)n_in; (void)out_size; (void)d_ws; (void)ws_size;
  dilated_conv_kernel<<<dim3(256), dim3(256), 0, stream>>>((const unsigned short*)d_in[0], (const unsigned short*)d_in[1],
                                                          (unsigned short*)d_out);
}
```
